```python
import torch

def _softmax(x, axis=-1):
    return torch.softmax(x, dim=axis)

def _gelu(x):
    return torch.nn.functional.gelu(x, approximate='tanh')

def _silu(x):
    return torch.nn.functional.silu(x)

def _sigmoid(x):
    return torch.sigmoid(x)

def _layernorm(x, axis=-1, eps=1e-05):
    m = x.mean(dim=axis, keepdim=True)
    v = x.var(dim=axis, keepdim=True, unbiased=False)
    return (x - m) / torch.sqrt(v + eps)

def _scatter_add(tgt, idx, src, axis=0):
    return tgt.clone().index_add_(axis, idx, src)

def _segment_sum(src, seg, n):
    out = torch.zeros((n,) + src.shape[1:], dtype=src.dtype)
    return out.index_add_(0, seg, src)

def _pad(x, pw):
    flat = tuple((v for pair in reversed(pw) for v in pair))
    return torch.nn.functional.pad(x, flat)
kernel_name = 'standalone__tree_speculative_verify_tree_attention_over_candidate_token_tree__decode'

def setup_inputs():

    _w = lambda a: (lambda t: t.to(torch.bfloat16) if t.is_floating_point() else t)(torch.as_tensor(a))
    B, Hq, Hkv, T, C, D = (1, 32, 32, 1, 8192, 128)
    tree_mask = torch.zeros((B, T, T), dtype=float)
    for b in range(B):
        parent = torch.zeros(T, dtype=torch.int64)
        for i in range(1, T):
            parent[i] = int(torch.randint(0, i, (1,)).item())
        for i in range(T):
            j = i
            tree_mask[b, i, j] = 1.0
            while j > 0:
                j = parent[j]
                tree_mask[b, i, j] = 1.0
    return {'q': _w((lambda *s: torch.randn(*s, dtype=torch.bfloat16) * 0.8)(B, Hq, T, D)), 'k_cache': _w((lambda *s: torch.randn(*s, dtype=torch.bfloat16) * 0.8)(B, Hkv, C, D)), 'v_cache': _w((lambda *s: torch.randn(*s, dtype=torch.bfloat16) * 0.05)(B, Hkv, C, D)), 'k_tree': _w((lambda *s: torch.randn(*s, dtype=torch.bfloat16) * 0.8)(B, Hkv, T, D)), 'v_tree': _w((lambda *s: torch.randn(*s, dtype=torch.bfloat16) * 0.05)(B, Hkv, T, D)), 'tree_mask': _w(tree_mask)}

def reference(q, k_cache, v_cache, k_tree, v_tree, tree_mask):
    B, Hq, T, D = q.shape
    Hkv, C = (k_cache.shape[1], k_cache.shape[2])
    rep = Hq // Hkv
    kc = (lambda x, n, axis: torch.repeat_interleave(x, n, dim=axis))(k_cache, rep, axis=1)
    vc = (lambda x, n, axis: torch.repeat_interleave(x, n, dim=axis))(v_cache, rep, axis=1)
    kt = (lambda x, n, axis: torch.repeat_interleave(x, n, dim=axis))(k_tree, rep, axis=1)
    vt = (lambda x, n, axis: torch.repeat_interleave(x, n, dim=axis))(v_tree, rep, axis=1)
    k = torch.concatenate([kc, kt], axis=2)
    v = torch.concatenate([vc, vt], axis=2)
    scale = D ** (-0.5)
    scores = torch.einsum('bhqd,bhkd->bhqk', q, k) * scale
    ctx_mask = torch.ones((B, 1, T, C), dtype=scores.dtype)
    tm = tree_mask[:, None, :, :].clone()
    full_mask = torch.concatenate([ctx_mask, tm], axis=-1)
    scores = scores + (1.0 - full_mask) * -1000000000.0
    p = _softmax(scores, axis=-1)
    return torch.einsum('bhqk,bhkd->bhqd', p, v)
```

```cpp
#include <hip/hip_runtime.h>
#include <hip/hip_bf16.h>
#include <cstdint>

namespace {

constexpr int H = 32, C = 8192, D = 128;
constexpr int NSPLIT = 8, WAVES = 4;
constexpr int NW = NSPLIT * WAVES;
constexpr int NT = C / 16;
constexpr int NTW = NT / NW;
constexpr int NG = NTW / 4;
constexpr int AHEAD = 4, RING = AHEAD * 4;
constexpr int ROT = 16;
constexpr int PACE = 900;
constexpr int STAG = PACE / 4;
constexpr int LEAD = 2;
constexpr int WIN = 12;
constexpr float SCALE = 0.088388347648318440f;
constexpr float LOG2E = 1.4426950408889634f;
constexpr unsigned OOB = 0x7fff0000u;
constexpr unsigned GR_OFF = 65536, GR_ACC = 1024, GR_HEAD = GR_ACC + NSPLIT * 1024;

using bf16x2 = __attribute__((ext_vector_type(2))) __bf16;
using bf16x8 = __attribute__((ext_vector_type(8))) __bf16;
using u32x4 = __attribute__((ext_vector_type(4))) unsigned;
using f32x4 = __attribute__((ext_vector_type(4))) float;
using f32x2 = __attribute__((ext_vector_type(2))) float;
#define SB() __builtin_amdgcn_sched_barrier(0)
#define WG_BARRIER() asm volatile("s_waitcnt lgkmcnt(0)\n\ts_barrier" ::: "memory")

__device__ __forceinline__ float dot2(unsigned k, unsigned q, float acc) {
  return __builtin_amdgcn_fdot2_f32_bf16(__builtin_bit_cast(bf16x2, k), __builtin_bit_cast(bf16x2, q), acc, false);
}
__device__ __forceinline__ float dot8(u32x4 k, u32x4 q) {
  return dot2(k[3], q[3], dot2(k[2], q[2], dot2(k[1], q[1], dot2(k[0], q[0], 0.f))));
}
template <int CTRL> __device__ __forceinline__ float dpp(float x) {
  return __builtin_bit_cast(float, __builtin_amdgcn_mov_dpp(__builtin_bit_cast(int, x), CTRL, 0xf, 0xf, true));
}
template <int CTRL> __device__ __forceinline__ unsigned dppu(unsigned x) {
  return (unsigned)__builtin_amdgcn_mov_dpp((int)x, CTRL, 0xf, 0xf, true);
}
constexpr int XOR1 = 0xB1, XOR2 = 0x4E, XOR7 = 0x141, XOR8 = 0x128;
__device__ __forceinline__ float wave_max(float x) {
  x = fmaxf(x, dpp<XOR1>(x)); x = fmaxf(x, dpp<XOR2>(x)); x = fmaxf(x, dpp<XOR7>(x)); x = fmaxf(x, dpp<XOR8>(x));
  auto s = __builtin_amdgcn_permlane16_swap(__float_as_uint(x), __float_as_uint(x), false, false);
  x = fmaxf(__uint_as_float(s[0]), __uint_as_float(s[1]));
  auto t = __builtin_amdgcn_permlane32_swap(__float_as_uint(x), __float_as_uint(x), false, false);
  return fmaxf(__uint_as_float(t[0]), __uint_as_float(t[1]));
}
__device__ __forceinline__ float wave_sum(float x) {
  x += dpp<XOR1>(x); x += dpp<XOR2>(x); x += dpp<XOR7>(x); x += dpp<XOR8>(x);
  auto s = __builtin_amdgcn_permlane16_swap(__float_as_uint(x), __float_as_uint(x), false, false);
  x = __uint_as_float(s[0]) + __uint_as_float(s[1]);
  auto t = __builtin_amdgcn_permlane32_swap(__float_as_uint(x), __float_as_uint(x), false, false);
  return __uint_as_float(t[0]) + __uint_as_float(t[1]);
}
__device__ __forceinline__ unsigned bf16_rne(float x) {
  return (unsigned)__builtin_bit_cast(unsigned short, (__bf16)x);
}
__device__ __forceinline__ unsigned pack_p(float p) {
  const unsigned hi = bf16_rne(p);
  const unsigned lo = bf16_rne(p - __builtin_bit_cast(float, hi << 16));
  return hi | (lo << 16);
}
__device__ __forceinline__ u32x4 srd(const void* p, unsigned bytes) {
  const unsigned long long a = (unsigned long long)p;
  return u32x4{(unsigned)a, (unsigned)(a >> 32) & 0xffffu, bytes, 0x00020000u};
}
template <int OFF> __device__ __forceinline__ void ld_nt(u32x4& r, unsigned voff, u32x4 rs, unsigned soff) {
  asm volatile("buffer_load_dwordx4 %0, %1, %2, %3 offen offset:%c4 nt" : "+v"(r) : "v"(voff), "s"(rs), "s"(soff), "i"(OFF) : "memory");
}
__device__ __forceinline__ void ld_sc1(u32x4& r, unsigned voff, u32x4 rs, unsigned soff) {
  asm volatile("buffer_load_dwordx4 %0, %1, %2, %3 offen sc1" : "+v"(r) : "v"(voff), "s"(rs), "s"(soff) : "memory");
}
__device__ __forceinline__ void st_sc1(u32x4 x, unsigned voff, u32x4 rs, unsigned soff) {
  asm volatile("buffer_store_dwordx4 %0, %1, %2, %3 offen sc1\n\ts_nop 1" :: "v"(x), "v"(voff), "s"(rs), "s"(soff) : "memory");
}
template <int N> __device__ __forceinline__ void wait4(u32x4& r) { asm volatile("s_waitcnt vmcnt(%c1)" : "+v"(r) : "i"(N) : "memory"); }
__device__ __forceinline__ void pin3(unsigned& a, float& b, float& c) { asm volatile("" : "+v"(a), "+v"(b), "+v"(c)); }
__device__ __forceinline__ void undef4(u32x4& r) { asm volatile("" : "=v"(r)); }
__device__ __forceinline__ void keep4(u32x4& r) { asm volatile("" : "+v"(r)); }

constexpr int brev4(int t) { return ((t & 1) << 3) | ((t & 2) << 1) | ((t & 4) >> 1) | ((t & 8) >> 3); }
template <int LN, int N> __device__ __forceinline__ void pv_step(f32x4& acc, unsigned pk, u32x4& v, const unsigned (&sel)[4]) {
  const unsigned pb = dppu<0x150 + LN>(pk);
  u32x4 b;
#pragma unroll
  for (int w = 0; w < 4; ++w) b[w] = __builtin_amdgcn_perm(pb, pb, sel[w]);
  keep4(b);
  wait4<N>(v);
  acc = __builtin_amdgcn_mfma_f32_16x16x32_bf16(__builtin_bit_cast(bf16x8, v), __builtin_bit_cast(bf16x8, b), acc, 0, 0, 0);
  SB();
}
template <int T> struct IC { static constexpr int v = T; };
template <int N, class F> __device__ __forceinline__ void sfor(F&& f) {
  if constexpr (N > 0) { sfor<N - 1>(f); f(IC<N - 1>{}); }
}

}

__global__ void __launch_bounds__(WAVES * 64)
tree_attn_decode_kernel(const void* q, const void* kc, const void* vc, unsigned char* ws, const void* kt, const void* vt,
                        const void* mask, void* out) {
  __shared__ __attribute__((aligned(16))) float lds[WAVES * 128 + WAVES * NG * 64 + 8];
  const int s = blockIdx.x % NSPLIT, h = blockIdx.x / NSPLIT;
  const int tid = threadIdx.x, lane = tid & 63, wave = __builtin_amdgcn_readfirstlane(tid >> 6), c = lane & 15;
  const int w = s * WAVES + wave;
  const unsigned hb = (unsigned)h * (C * D * 2);
  const unsigned voff = lane * 16;
  auto toffs = [&](int i) -> unsigned { return hb + (unsigned)((i * NW + w + h * ROT) & (NT - 1)) * 4096u; };
  const u32x4 krs = srd(kc, H * C * D * 2), vrs = srd(vc, H * C * D * 2);
  u32x4 qv, r[RING];
  asm volatile("buffer_load_dwordx4 %0, %1, %2, %3 offen" : "=v"(qv) : "v"(c * 16), "s"(srd(q, H * D * 2)), "s"(h * (D * 2)) : "memory");
  const unsigned tz = (unsigned)__builtin_amdgcn_s_memrealtime() + ((wave * STAG) >> 4);
  auto pace = [&](int k) {
    asm volatile("s_waitcnt vmcnt(%c0)" :: "i"(WIN - 4) : "memory");
    const unsigned due = tz + (unsigned)(((k - LEAD + 1) * PACE) >> 4);
    while ((int)((unsigned)__builtin_amdgcn_s_memrealtime() - due) < 0) __builtin_amdgcn_s_sleep(1);
  };
  sfor<LEAD * 4>([&](auto I) { undef4(r[I.v]); ld_nt<(I.v & 3) * 1024>(r[I.v], voff, krs, toffs(I.v >> 2)); });
  const bool cons = s == 0 && wave == 0;
  const u32x4 ers = srd(ws + 512 * h, 8);
  unsigned long long epoch;
  u32x4 ktv, ml;
  unsigned vtv, mkv;
  asm volatile("buffer_load_dwordx2 %0, off, %1, 0 sc1" : "=v"(epoch) : "s"(ers) : "memory");
  asm volatile("" : "=v"(ktv), "=v"(ml), "=v"(vtv), "=v"(mkv));
  if (cons) {
    asm volatile("buffer_load_dwordx4 %0, %1, %2, %3 offen" : "+v"(ktv) : "v"(c * 16), "s"(srd(kt, H * D * 2)), "s"(h * (D * 2)) : "memory");
    asm volatile("buffer_load_dword %0, %1, %2, %3 offen" : "+v"(vtv) : "v"(lane * 4), "s"(srd(vt, H * D * 2)), "s"(h * (D * 2)) : "memory");
    asm volatile("buffer_load_ushort %0, off, %1, 0" : "+v"(mkv) : "s"(srd(mask, 2)) : "memory");
  }
  sfor<RING - LEAD * 4>([&](auto I) {
    constexpr int i = I.v + LEAD * 4;
    if constexpr ((i & 3) == 0) pace(i >> 2);
    undef4(r[i]);
    ld_nt<(i & 3) * 1024>(r[i], voff, krs, toffs(i >> 2));
  });
  asm volatile("s_waitcnt vmcnt(%c1)" : "+v"(qv) : "i"(RING) : "memory");
  float* sco = lds + WAVES * 128 + wave * (NG * 64) + lane;
  float* wmx = lds + WAVES * 128 + WAVES * NG * 64;
  float mx = -INFINITY;
#pragma unroll 1
  for (int g = 0; g < NG; ++g) {
    float xa = 0.f, ya = 0.f, za = 0.f, ua = 0.f, sc = 0.f;
    sfor<16>([&](auto I) {
      constexpr int t = I.v;
      wait4<RING - 1>(r[t % RING]);
      float d = dot8(r[t % RING], qv);
      asm volatile("" : "+v"(d));
      const int ti = g * 4 + (t >> 2) + AHEAD;
      const bool isv = ti >= NTW;
      if constexpr ((t & 3) == 0) pace(ti);
      ld_nt<(t & 3) * 1024>(r[t % RING], voff, isv ? vrs : krs, toffs(isv ? ti - NTW : ti));
      d += dpp<XOR8>(d);
      if constexpr (!(t & 1)) xa = d;
      else {
        float e = (c & 8) ? d : xa;
        e += dpp<XOR7>(e);
        if constexpr (!(t & 2)) ya = e;
        else {
          e = (c & 4) ? e : ya;
          e += dpp<XOR2>(e);
          if constexpr (!(t & 4)) za = e;
          else {
            e = (c & 2) ? e : za;
            e += dpp<XOR1>(e);
            if constexpr (!(t & 8)) ua = e;
            else sc = ((c & 1) ? e : ua) * (SCALE * LOG2E);
          }
        }
      }
    });
    mx = fmaxf(mx, sc);
    sco[g * 64] = sc;
  }
  asm volatile("" : "+v"(epoch), "+v"(ktv), "+v"(vtv), "+v"(mkv));
  mx = wave_max(mx);
  mx = (mx == -INFINITY) ? -3.0e38f : mx;
  float l = 0.f;
#pragma unroll
  for (int g = 0; g < NG; ++g) l += __builtin_amdgcn_exp2f(sco[g * 64] - mx);
  l = wave_sum(l);
  if (lane == 0) { wmx[wave] = mx; wmx[4 + wave] = l; }
  const unsigned tagA = (unsigned)(epoch + 1ull), tagB = (unsigned)((epoch + 1ull) >> 32) ^ (tagA * 0x9E3779B1u);
  const u32x4 gsr = srd(ws + GR_OFF + h * GR_HEAD, GR_HEAD);
  unsigned sel[4];
  {
    const unsigned b = (c & 8) ? 0x0302u : 0x0100u;
    const unsigned sv = (c & 1) ? ((b << 16) | 0x0c0cu) : (0x0c0c0000u | b);
#pragma unroll
    for (int i = 0; i < 4; ++i) sel[i] = (((c >> 1) & 3) == i) ? sv : 0x0c0c0c0cu;
  }
  f32x4 acc = {0.f, 0.f, 0.f, 0.f};
#pragma unroll 1
  for (int g = 0; g < NG - 1; ++g) {
    const unsigned pk = pack_p(__builtin_amdgcn_exp2f(sco[g * 64] - mx));
    sfor<16>([&](auto I) {
      constexpr int t = I.v;
      pv_step<brev4(t), RING - 1>(acc, pk, r[t % RING], sel);
      const int ti = g * 4 + (t >> 2) + AHEAD;
      if constexpr ((t & 3) == 0) pace(NTW + ti);
      ld_nt<(t & 3) * 1024>(r[t % RING], voff, vrs, toffs(ti));
    });
  }
  {
    const float sl = sco[(NG - 1) * 64];
    WG_BARRIER();
    const f32x4 m4 = *reinterpret_cast<const f32x4*>(wmx), l4 = *reinterpret_cast<const f32x4*>(wmx + 4);
    const float M = fmaxf(fmaxf(m4[0], m4[1]), fmaxf(m4[2], m4[3]));
    float L = 0.f;
#pragma unroll
    for (int i = 0; i < WAVES; ++i) L = fmaf(__builtin_amdgcn_exp2f(m4[i] - M), l4[i], L);
    st_sc1(u32x4{tagA, __float_as_uint(M), tagB, __float_as_uint(L)}, tid == 0 ? 0u : OOB, gsr, s * 16);
    acc *= __builtin_amdgcn_exp2f(mx - M);
    const unsigned pk = pack_p(__builtin_amdgcn_exp2f(sl - M));
    sfor<16>([&](auto I) { pv_step<brev4(I.v), 16 - I.v>(acc, pk, r[I.v], sel); });
  }
  {
    const bool up = c & 8;
    const float s0 = up ? acc[0] : acc[2], s1 = up ? acc[1] : acc[3];
    const float e0 = (up ? acc[2] : acc[0]) + dpp<XOR8>(s0), e1 = (up ? acc[3] : acc[1]) + dpp<XOR8>(s1);
    float* rec = lds + wave * 128 + 32 * (lane >> 4) + 2 * (c & 8) + (c & 7);
    rec[0] = e0; rec[8] = e1;
  }
  u32x4 av[NSPLIT], bv[NSPLIT];
  sfor<NSPLIT>([&](auto J) { undef4(av[J.v]); undef4(bv[J.v]); });
  auto sweep = [&](u32x4 (&x)[NSPLIT]) {
    sfor<NSPLIT - 1>([&](auto J) { ld_sc1(x[J.v + 1], voff, gsr, GR_ACC + (J.v + 1) * 1024); });
  };
  if (cons) { ld_sc1(ml, (lane & (NSPLIT - 1)) * 16, gsr, 0); sweep(av); }
  WG_BARRIER();
  if (wave != 0) return;
  f32x2 o = *reinterpret_cast<const f32x2*>(lds + 2 * lane);
#pragma unroll
  for (int i = 1; i < WAVES; ++i) o += *reinterpret_cast<const f32x2*>(lds + i * 128 + 2 * lane);
  if (s != 0) {
    st_sc1(u32x4{tagA, __float_as_uint(o[0]), tagB, __float_as_uint(o[1])}, voff, gsr, GR_ACC + s * 1024);
    return;
  }
  float dt = dot8(ktv, qv);
  dt += dpp<XOR1>(dt); dt += dpp<XOR2>(dt); dt += dpp<XOR7>(dt); dt += dpp<XOR8>(dt);
  const float mk = __builtin_bit_cast(float, mkv << 16);
  const float st = (dt * SCALE + (1.0f - mk) * -1000000000.0f) * LOG2E;
  wait4<NSPLIT - 1>(ml);
  {
    unsigned spins = 0;
    while (__builtin_expect(!__all((ml[0] == tagA) & (ml[2] == tagB)), 0) && ++spins < (1u << 18)) {
      ld_sc1(ml, (lane & (NSPLIT - 1)) * 16, gsr, 0);
      wait4<0>(ml);
    }
  }
  const float mj = __uint_as_float(ml[1]);
  float Mf = fmaxf(mj, st);
  Mf = fmaxf(Mf, dpp<XOR1>(Mf)); Mf = fmaxf(Mf, dpp<XOR2>(Mf)); Mf = fmaxf(Mf, dpp<XOR7>(Mf));
  float wj = __builtin_amdgcn_exp2f(mj - Mf), wt = __builtin_amdgcn_exp2f(st - Mf);
  float Lf = wj * __uint_as_float(ml[3]);
  Lf += dpp<XOR1>(Lf); Lf += dpp<XOR2>(Lf); Lf += dpp<XOR7>(Lf);
  const float inv = 1.0f / (Lf + wt);
  wj *= inv; wt *= inv;
  float wsp[NSPLIT];
#pragma unroll
  for (int j = 0; j < NSPLIT; ++j) wsp[j] = __uint_as_float(__builtin_amdgcn_readlane(__float_as_uint(wj), j));
  const float t0 = wt * __builtin_bit_cast(float, vtv << 16), t1 = wt * __builtin_bit_cast(float, vtv & 0xffff0000u);
  const float b0 = fmaf(wsp[0], o[0], t0), b1 = fmaf(wsp[0], o[1], t1);
  float f0 = 0.f, f1 = 0.f;
  auto take = [&](u32x4 (&x)[NSPLIT]) -> bool {
    unsigned bad = 0;
    f0 = b0; f1 = b1;
    sfor<NSPLIT - 1>([&](auto J) {
      constexpr int j = J.v + 1;
      wait4<2 * (NSPLIT - 1) - j>(x[j]);
      bad |= (x[j][0] ^ tagA) | (x[j][2] ^ tagB);
      f0 = fmaf(wsp[j], __uint_as_float(x[j][1]), f0);
      f1 = fmaf(wsp[j], __uint_as_float(x[j][3]), f1);
      pin3(bad, f0, f1);
    });
    return !__any(bad != 0);
  };
  for (unsigned spins = 0; spins < (1u << 17); ++spins) {
    sweep(bv);
    if (take(av)) break;
    sweep(av);
    if (take(bv)) break;
  }
  asm volatile("buffer_store_dword %0, %1, %2, %3 offen sc1"
               :: "v"(bf16_rne(f0) | (bf16_rne(f1) << 16)), "v"(lane * 4), "s"(srd(out, H * D * 2)), "s"(h * (D * 2)) : "memory");
  asm volatile("buffer_store_dwordx2 %0, %1, %2, 0 offen sc1" :: "v"(epoch + 1ull), "v"(lane == 0 ? 0u : OOB), "s"(ers) : "memory");
  sfor<NSPLIT - 1>([&](auto J) { keep4(av[J.v + 1]); keep4(bv[J.v + 1]); });
}

extern "C" void kernel_launch(void* const* d_in, const int* in_sizes, int n_in, void* d_out, int out_size, void* d_ws,
                              size_t ws_size, hipStream_t stream) {
  tree_attn_decode_kernel<<<H * NSPLIT, WAVES * 64, 0, stream>>>(d_in[0], d_in[1], d_in[2], (unsigned char*)d_ws, d_in[3],
                                                                 d_in[4], d_in[5], d_out);
}
```

```cpp
#include <hip/hip_runtime.h>
#include <hip/hip_bf16.h>
#include <cstdint>

namespace {

constexpr int H = 32, C = 8192, D = 128;
constexpr int NSPLIT = 8, WAVES = 4;
constexpr int NW = NSPLIT * WAVES;
constexpr int NT = C / 16;
constexpr int NTW = NT / NW;
constexpr int NG = NTW / 4;
constexpr int AHEAD = 4, RING = AHEAD * 4;
constexpr int ROT = 16;
constexpr int PACE = 905;
constexpr int STAG = PACE / 4;
constexpr int LEAD = 2;
constexpr int WIN = 12;
constexpr float SCALE = 0.088388347648318440f;
constexpr float LOG2E = 1.4426950408889634f;
constexpr unsigned OOB = 0x7fff0000u;
constexpr unsigned GR_OFF = 65536, GR_TIME = 512, GR_ACC = 1024, GR_HEAD = GR_ACC + NSPLIT * 1024;

using bf16x2 = __attribute__((ext_vector_type(2))) __bf16;
using bf16x8 = __attribute__((ext_vector_type(8))) __bf16;
using u32x4 = __attribute__((ext_vector_type(4))) unsigned;
using f32x4 = __attribute__((ext_vector_type(4))) float;
using f32x2 = __attribute__((ext_vector_type(2))) float;
#define SB() __builtin_amdgcn_sched_barrier(0)
#define WG_BARRIER() asm volatile("s_waitcnt lgkmcnt(0)\n\ts_barrier" ::: "memory")

__device__ __forceinline__ float dot2(unsigned k, unsigned q, float acc) {
  return __builtin_amdgcn_fdot2_f32_bf16(__builtin_bit_cast(bf16x2, k), __builtin_bit_cast(bf16x2, q), acc, false);
}
__device__ __forceinline__ float dot8(u32x4 k, u32x4 q) {
  return dot2(k[3], q[3], dot2(k[2], q[2], dot2(k[1], q[1], dot2(k[0], q[0], 0.f))));
}
template <int CTRL> __device__ __forceinline__ float dpp(float x) {
  return __builtin_bit_cast(float, __builtin_amdgcn_mov_dpp(__builtin_bit_cast(int, x), CTRL, 0xf, 0xf, true));
}
template <int CTRL> __device__ __forceinline__ unsigned dppu(unsigned x) {
  return (unsigned)__builtin_amdgcn_mov_dpp((int)x, CTRL, 0xf, 0xf, true);
}
constexpr int XOR1 = 0xB1, XOR2 = 0x4E, XOR7 = 0x141, XOR8 = 0x128;
__device__ __forceinline__ float wave_max(float x) {
  x = fmaxf(x, dpp<XOR1>(x)); x = fmaxf(x, dpp<XOR2>(x)); x = fmaxf(x, dpp<XOR7>(x)); x = fmaxf(x, dpp<XOR8>(x));
  auto s = __builtin_amdgcn_permlane16_swap(__float_as_uint(x), __float_as_uint(x), false, false);
  x = fmaxf(__uint_as_float(s[0]), __uint_as_float(s[1]));
  auto t = __builtin_amdgcn_permlane32_swap(__float_as_uint(x), __float_as_uint(x), false, false);
  return fmaxf(__uint_as_float(t[0]), __uint_as_float(t[1]));
}
__device__ __forceinline__ float wave_sum(float x) {
  x += dpp<XOR1>(x); x += dpp<XOR2>(x); x += dpp<XOR7>(x); x += dpp<XOR8>(x);
  auto s = __builtin_amdgcn_permlane16_swap(__float_as_uint(x), __float_as_uint(x), false, false);
  x = __uint_as_float(s[0]) + __uint_as_float(s[1]);
  auto t = __builtin_amdgcn_permlane32_swap(__float_as_uint(x), __float_as_uint(x), false, false);
  return __uint_as_float(t[0]) + __uint_as_float(t[1]);
}
__device__ __forceinline__ unsigned bf16_rne(float x) {
  return (unsigned)__builtin_bit_cast(unsigned short, (__bf16)x);
}
__device__ __forceinline__ unsigned pack_p(float p) {
  const unsigned hi = bf16_rne(p);
  const unsigned lo = bf16_rne(p - __builtin_bit_cast(float, hi << 16));
  return hi | (lo << 16);
}
__device__ __forceinline__ u32x4 srd(const void* p, unsigned bytes) {
  const unsigned long long a = (unsigned long long)p;
  return u32x4{(unsigned)a, (unsigned)(a >> 32) & 0xffffu, bytes, 0x00020000u};
}
template <int OFF> __device__ __forceinline__ void ld_nt(u32x4& r, unsigned voff, u32x4 rs, unsigned soff) {
  asm volatile("buffer_load_dwordx4 %0, %1, %2, %3 offen offset:%c4 nt" : "+v"(r) : "v"(voff), "s"(rs), "s"(soff), "i"(OFF) : "memory");
}
__device__ __forceinline__ void ld_sc1(u32x4& r, unsigned voff, u32x4 rs, unsigned soff) {
  asm volatile("buffer_load_dwordx4 %0, %1, %2, %3 offen sc1" : "+v"(r) : "v"(voff), "s"(rs), "s"(soff) : "memory");
}
__device__ __forceinline__ void st_sc1(u32x4 x, unsigned voff, u32x4 rs, unsigned soff) {
  asm volatile("buffer_store_dwordx4 %0, %1, %2, %3 offen sc1\n\ts_nop 1" :: "v"(x), "v"(voff), "s"(rs), "s"(soff) : "memory");
}
template <int N> __device__ __forceinline__ void wait4(u32x4& r) { asm volatile("s_waitcnt vmcnt(%c1)" : "+v"(r) : "i"(N) : "memory"); }
__device__ __forceinline__ void pin3(unsigned& a, float& b, float& c) { asm volatile("" : "+v"(a), "+v"(b), "+v"(c)); }
__device__ __forceinline__ void pin1(unsigned long long& a) { asm volatile("" : "+v"(a)); }
__device__ __forceinline__ unsigned tag_a(unsigned long long epoch) { return (unsigned)(epoch + 1ull); }
__device__ __forceinline__ unsigned tag_b(unsigned long long epoch) { return (unsigned)((epoch + 1ull) >> 32) ^ (tag_a(epoch) * 0x9E3779B1u); }
__device__ __forceinline__ void undef4(u32x4& r) { asm volatile("" : "=v"(r)); }
__device__ __forceinline__ void keep4(u32x4& r) { asm volatile("" : "+v"(r)); }

constexpr int brev4(int t) { return ((t & 1) << 3) | ((t & 2) << 1) | ((t & 4) >> 1) | ((t & 8) >> 3); }
template <int LN, int N> __device__ __forceinline__ void pv_step(f32x4& acc, unsigned pk, u32x4& v, const unsigned (&sel)[4]) {
  const unsigned pb = dppu<0x150 + LN>(pk);
  u32x4 b;
#pragma unroll
  for (int w = 0; w < 4; ++w) b[w] = __builtin_amdgcn_perm(pb, pb, sel[w]);
  keep4(b);
  wait4<N>(v);
  acc = __builtin_amdgcn_mfma_f32_16x16x32_bf16(__builtin_bit_cast(bf16x8, v), __builtin_bit_cast(bf16x8, b), acc, 0, 0, 0);
  SB();
}
template <int T> struct IC { static constexpr int v = T; };
template <int N, class F> __device__ __forceinline__ void sfor(F&& f) {
  if constexpr (N > 0) { sfor<N - 1>(f); f(IC<N - 1>{}); }
}

}

__global__ void __launch_bounds__(WAVES * 64)
tree_attn_decode_kernel(const void* q, const void* kc, const void* vc, unsigned char* ws, const void* kt, const void* vt,
                        const void* mask, void* out) {
  __shared__ __attribute__((aligned(16))) float lds[WAVES * 128 + WAVES * NG * 64 + 8];
  const int s = blockIdx.x % NSPLIT, h = blockIdx.x / NSPLIT;
  const int tid = threadIdx.x, lane = tid & 63, wave = __builtin_amdgcn_readfirstlane(tid >> 6), c = lane & 15;
  const int w = s * WAVES + wave;
  const unsigned hb = (unsigned)h * (C * D * 2);
  const unsigned voff = lane * 16;
  auto toffs = [&](int i) -> unsigned { return hb + (unsigned)((i * NW + w + h * ROT) & (NT - 1)) * 4096u; };
  const u32x4 krs = srd(kc, H * C * D * 2), vrs = srd(vc, H * C * D * 2);
  u32x4 qv, r[RING];
  asm volatile("buffer_load_dwordx4 %0, %1, %2, %3 offen" : "=v"(qv) : "v"(c * 16), "s"(srd(q, H * D * 2)), "s"(h * (D * 2)) : "memory");
  const unsigned tw = (unsigned)__builtin_amdgcn_s_memrealtime();
  unsigned tz = tw + ((wave * STAG) >> 4);
  auto pace = [&](int k) {
    asm volatile("s_waitcnt vmcnt(%c0)" :: "i"(WIN - 4) : "memory");
    const unsigned due = tz + (unsigned)(((k - LEAD + 1) * PACE) >> 4);
    while ((int)((unsigned)__builtin_amdgcn_s_memrealtime() - due) < 0) __builtin_amdgcn_s_sleep(1);
  };
  sfor<LEAD * 4>([&](auto I) { undef4(r[I.v]); ld_nt<(I.v & 3) * 1024>(r[I.v], voff, krs, toffs(I.v >> 2)); });
  const bool cons = s == 0 && wave == 0;
  const u32x4 ers = srd(ws + 512 * h, 8);
  unsigned long long epoch;
  u32x4 ktv, ml;
  unsigned vtv, mkv;
  asm volatile("buffer_load_dwordx2 %0, off, %1, 0 sc1" : "=v"(epoch) : "s"(ers) : "memory");
  asm volatile("" : "=v"(ktv), "=v"(ml), "=v"(vtv), "=v"(mkv));
  if (cons) {
    asm volatile("buffer_load_dwordx4 %0, %1, %2, %3 offen" : "+v"(ktv) : "v"(c * 16), "s"(srd(kt, H * D * 2)), "s"(h * (D * 2)) : "memory");
    asm volatile("buffer_load_dword %0, %1, %2, %3 offen" : "+v"(vtv) : "v"(lane * 4), "s"(srd(vt, H * D * 2)), "s"(h * (D * 2)) : "memory");
    asm volatile("buffer_load_ushort %0, off, %1, 0" : "+v"(mkv) : "s"(srd(mask, 2)) : "memory");
  }
  sfor<RING - LEAD * 4>([&](auto I) {
    constexpr int i = I.v + LEAD * 4;
    if constexpr ((i & 3) == 0) pace(i >> 2);
    undef4(r[i]);
    ld_nt<(i & 3) * 1024>(r[i], voff, krs, toffs(i >> 2));
  });
  asm volatile("s_waitcnt vmcnt(%c1)" : "+v"(qv) : "i"(RING) : "memory");
  const u32x4 gsr = srd(ws + GR_OFF + h * GR_HEAD, GR_HEAD);
  float* sco = lds + WAVES * 128 + wave * (NG * 64) + lane;
  float* wmx = lds + WAVES * 128 + WAVES * NG * 64;
  float mx = -INFINITY;
#pragma unroll 1
  for (int g = 0; g < NG; ++g) {
    float xa = 0.f, ya = 0.f, za = 0.f, ua = 0.f, sc = 0.f;
    sfor<16>([&](auto I) {
      constexpr int t = I.v;
      wait4<RING - 1>(r[t % RING]);
      if constexpr (t == LEAD * 4) {
        if (g == 0 && wave == 0) {
          pin1(epoch);
          st_sc1(u32x4{tag_a(epoch), tw, tag_b(epoch), 0u}, lane == 0 ? 0u : OOB, gsr, GR_TIME + s * 16);
        }
        if (g == 1) ld_sc1(ml, (lane & (NSPLIT - 1)) * 16, gsr, GR_TIME);
        if (g == 2) {
          keep4(ml); pin1(epoch);
          int dl = (int)(ml[1] - tw);
          dl = (ml[0] == tag_a(epoch) && ml[2] == tag_b(epoch) && dl < 0 && dl > -2000) ? dl : 0;
          dl = min(dl, (int)dppu<XOR1>((unsigned)dl)); dl = min(dl, (int)dppu<XOR2>((unsigned)dl)); dl = min(dl, (int)dppu<XOR7>((unsigned)dl));
          tz += (unsigned)__builtin_amdgcn_readfirstlane(dl);
        }
      }
      float d = dot8(r[t % RING], qv);
      asm volatile("" : "+v"(d));
      const int ti = g * 4 + (t >> 2) + AHEAD;
      const bool isv = ti >= NTW;
      if constexpr ((t & 3) == 0) pace(ti);
      ld_nt<(t & 3) * 1024>(r[t % RING], voff, isv ? vrs : krs, toffs(isv ? ti - NTW : ti));
      d += dpp<XOR8>(d);
      if constexpr (!(t & 1)) xa = d;
      else {
        float e = (c & 8) ? d : xa;
        e += dpp<XOR7>(e);
        if constexpr (!(t & 2)) ya = e;
        else {
          e = (c & 4) ? e : ya;
          e += dpp<XOR2>(e);
          if constexpr (!(t & 4)) za = e;
          else {
            e = (c & 2) ? e : za;
            e += dpp<XOR1>(e);
            if constexpr (!(t & 8)) ua = e;
            else sc = ((c & 1) ? e : ua) * (SCALE * LOG2E);
          }
        }
      }
    });
    mx = fmaxf(mx, sc);
    sco[g * 64] = sc;
  }
  asm volatile("" : "+v"(epoch), "+v"(ktv), "+v"(vtv), "+v"(mkv));
  mx = wave_max(mx);
  mx = (mx == -INFINITY) ? -3.0e38f : mx;
  float l = 0.f;
#pragma unroll
  for (int g = 0; g < NG; ++g) l += __builtin_amdgcn_exp2f(sco[g * 64] - mx);
  l = wave_sum(l);
  if (lane == 0) { wmx[wave] = mx; wmx[4 + wave] = l; }
  const unsigned tagA = tag_a(epoch), tagB = tag_b(epoch);
  unsigned sel[4];
  {
    const unsigned b = (c & 8) ? 0x0302u : 0x0100u;
    const unsigned sv = (c & 1) ? ((b << 16) | 0x0c0cu) : (0x0c0c0000u | b);
#pragma unroll
    for (int i = 0; i < 4; ++i) sel[i] = (((c >> 1) & 3) == i) ? sv : 0x0c0c0c0cu;
  }
  f32x4 acc = {0.f, 0.f, 0.f, 0.f};
#pragma unroll 1
  for (int g = 0; g < NG - 1; ++g) {
    const unsigned pk = pack_p(__builtin_amdgcn_exp2f(sco[g * 64] - mx));
    sfor<16>([&](auto I) {
      constexpr int t = I.v;
      pv_step<brev4(t), RING - 1>(acc, pk, r[t % RING], sel);
      const int ti = g * 4 + (t >> 2) + AHEAD;
      if constexpr ((t & 3) == 0) pace(NTW + ti);
      ld_nt<(t & 3) * 1024>(r[t % RING], voff, vrs, toffs(ti));
    });
  }
  {
    const float sl = sco[(NG - 1) * 64];
    WG_BARRIER();
    const f32x4 m4 = *reinterpret_cast<const f32x4*>(wmx), l4 = *reinterpret_cast<const f32x4*>(wmx + 4);
    const float M = fmaxf(fmaxf(m4[0], m4[1]), fmaxf(m4[2], m4[3]));
    float L = 0.f;
#pragma unroll
    for (int i = 0; i < WAVES; ++i) L = fmaf(__builtin_amdgcn_exp2f(m4[i] - M), l4[i], L);
    st_sc1(u32x4{tagA, __float_as_uint(M), tagB, __float_as_uint(L)}, tid == 0 ? 0u : OOB, gsr, s * 16);
    acc *= __builtin_amdgcn_exp2f(mx - M);
    const unsigned pk = pack_p(__builtin_amdgcn_exp2f(sl - M));
    sfor<16>([&](auto I) { pv_step<brev4(I.v), 16 - I.v>(acc, pk, r[I.v], sel); });
  }
  {
    const bool up = c & 8;
    const float s0 = up ? acc[0] : acc[2], s1 = up ? acc[1] : acc[3];
    const float e0 = (up ? acc[2] : acc[0]) + dpp<XOR8>(s0), e1 = (up ? acc[3] : acc[1]) + dpp<XOR8>(s1);
    float* rec = lds + wave * 128 + 32 * (lane >> 4) + 2 * (c & 8) + (c & 7);
    rec[0] = e0; rec[8] = e1;
  }
  u32x4 av[NSPLIT], bv[NSPLIT];
  sfor<NSPLIT>([&](auto J) { undef4(av[J.v]); undef4(bv[J.v]); });
  auto sweep = [&](u32x4 (&x)[NSPLIT]) {
    sfor<NSPLIT - 1>([&](auto J) { ld_sc1(x[J.v + 1], voff, gsr, GR_ACC + (J.v + 1) * 1024); });
  };
  if (cons) { ld_sc1(ml, (lane & (NSPLIT - 1)) * 16, gsr, 0); sweep(av); }
  WG_BARRIER();
  if (wave != 0) return;
  f32x2 o = *reinterpret_cast<const f32x2*>(lds + 2 * lane);
#pragma unroll
  for (int i = 1; i < WAVES; ++i) o += *reinterpret_cast<const f32x2*>(lds + i * 128 + 2 * lane);
  if (s != 0) {
    st_sc1(u32x4{tagA, __float_as_uint(o[0]), tagB, __float_as_uint(o[1])}, voff, gsr, GR_ACC + s * 1024);
    return;
  }
  float dt = dot8(ktv, qv);
  dt += dpp<XOR1>(dt); dt += dpp<XOR2>(dt); dt += dpp<XOR7>(dt); dt += dpp<XOR8>(dt);
  const float mk = __builtin_bit_cast(float, mkv << 16);
  const float st = (dt * SCALE + (1.0f - mk) * -1000000000.0f) * LOG2E;
  wait4<NSPLIT - 1>(ml);
  {
    unsigned spins = 0;
    while (__builtin_expect(!__all((ml[0] == tagA) & (ml[2] == tagB)), 0) && ++spins < (1u << 18)) {
      ld_sc1(ml, (lane & (NSPLIT - 1)) * 16, gsr, 0);
      wait4<0>(ml);
    }
  }
  const float mj = __uint_as_float(ml[1]);
  float Mf = fmaxf(mj, st);
  Mf = fmaxf(Mf, dpp<XOR1>(Mf)); Mf = fmaxf(Mf, dpp<XOR2>(Mf)); Mf = fmaxf(Mf, dpp<XOR7>(Mf));
  float wj = __builtin_amdgcn_exp2f(mj - Mf), wt = __builtin_amdgcn_exp2f(st - Mf);
  float Lf = wj * __uint_as_float(ml[3]);
  Lf += dpp<XOR1>(Lf); Lf += dpp<XOR2>(Lf); Lf += dpp<XOR7>(Lf);
  const float inv = 1.0f / (Lf + wt);
  wj *= inv; wt *= inv;
  float wsp[NSPLIT];
#pragma unroll
  for (int j = 0; j < NSPLIT; ++j) wsp[j] = __uint_as_float(__builtin_amdgcn_readlane(__float_as_uint(wj), j));
  const float t0 = wt * __builtin_bit_cast(float, vtv << 16), t1 = wt * __builtin_bit_cast(float, vtv & 0xffff0000u);
  const float b0 = fmaf(wsp[0], o[0], t0), b1 = fmaf(wsp[0], o[1], t1);
  float f0 = 0.f, f1 = 0.f;
  auto take = [&](u32x4 (&x)[NSPLIT]) -> bool {
    unsigned bad = 0;
    f0 = b0; f1 = b1;
    sfor<NSPLIT - 1>([&](auto J) {
      constexpr int j = J.v + 1;
      wait4<2 * (NSPLIT - 1) - j>(x[j]);
      bad |= (x[j][0] ^ tagA) | (x[j][2] ^ tagB);
      f0 = fmaf(wsp[j], __uint_as_float(x[j][1]), f0);
      f1 = fmaf(wsp[j], __uint_as_float(x[j][3]), f1);
      pin3(bad, f0, f1);
    });
    return !__any(bad != 0);
  };
  for (unsigned spins = 0; spins < (1u << 17); ++spins) {
    sweep(bv);
    if (take(av)) break;
    sweep(av);
    if (take(bv)) break;
  }
  asm volatile("buffer_store_dword %0, %1, %2, %3 offen sc1"
               :: "v"(bf16_rne(f0) | (bf16_rne(f1) << 16)), "v"(lane * 4), "s"(srd(out, H * D * 2)), "s"(h * (D * 2)) : "memory");
  asm volatile("buffer_store_dwordx2 %0, %1, %2, 0 offen sc1" :: "v"(epoch + 1ull), "v"(lane == 0 ? 0u : OOB), "s"(ers) : "memory");
  sfor<NSPLIT - 1>([&](auto J) { keep4(av[J.v + 1]); keep4(bv[J.v + 1]); });
}

extern "C" void kernel_launch(void* const* d_in, const int* in_sizes, int n_in, void* d_out, int out_size, void* d_ws,
                              size_t ws_size, hipStream_t stream) {
  tree_attn_decode_kernel<<<H * NSPLIT, WAVES * 64, 0, stream>>>(d_in[0], d_in[1], d_in[2], (unsigned char*)d_ws, d_in[3],
                                                                 d_in[4], d_in[5], d_out);
}
```

```cpp
#include <hip/hip_runtime.h>
#include <hip/hip_bf16.h>
#include <cstdint>

namespace {

constexpr int H = 32, C = 8192, D = 128;
constexpr int NSPLIT = 8, WAVES = 4;
constexpr int NW = NSPLIT * WAVES;
constexpr int NT = C / 16;
constexpr int NTW = NT / NW;
constexpr int NG = NTW / 4;
constexpr int AHEAD = 4, RING = AHEAD * 4;
constexpr int ROT = 16;
constexpr int PACE = 895;
constexpr int STAG = PACE / 4;
constexpr int LEAD = 2;
constexpr int WIN = 12;
constexpr float SCALE = 0.088388347648318440f;
constexpr float LOG2E = 1.4426950408889634f;
constexpr unsigned OOB = 0x7fff0000u;
constexpr unsigned GR_OFF = 65536, GR_TIME = 512, GR_ACC = 1024, GR_HEAD = GR_ACC + NSPLIT * 1024;

using bf16x2 = __attribute__((ext_vector_type(2))) __bf16;
using bf16x8 = __attribute__((ext_vector_type(8))) __bf16;
using u32x4 = __attribute__((ext_vector_type(4))) unsigned;
using f32x4 = __attribute__((ext_vector_type(4))) float;
using f32x2 = __attribute__((ext_vector_type(2))) float;
#define SB() __builtin_amdgcn_sched_barrier(0)
#define WG_BARRIER() asm volatile("s_waitcnt lgkmcnt(0)\n\ts_barrier" ::: "memory")

__device__ __forceinline__ float dot2(unsigned k, unsigned q, float acc) {
  return __builtin_amdgcn_fdot2_f32_bf16(__builtin_bit_cast(bf16x2, k), __builtin_bit_cast(bf16x2, q), acc, false);
}
__device__ __forceinline__ float dot8(u32x4 k, u32x4 q) {
  return dot2(k[3], q[3], dot2(k[2], q[2], dot2(k[1], q[1], dot2(k[0], q[0], 0.f))));
}
template <int CTRL> __device__ __forceinline__ float dpp(float x) {
  return __builtin_bit_cast(float, __builtin_amdgcn_mov_dpp(__builtin_bit_cast(int, x), CTRL, 0xf, 0xf, true));
}
template <int CTRL> __device__ __forceinline__ unsigned dppu(unsigned x) {
  return (unsigned)__builtin_amdgcn_mov_dpp((int)x, CTRL, 0xf, 0xf, true);
}
constexpr int XOR1 = 0xB1, XOR2 = 0x4E, XOR7 = 0x141, XOR8 = 0x128;
__device__ __forceinline__ float wave_max(float x) {
  x = fmaxf(x, dpp<XOR1>(x)); x = fmaxf(x, dpp<XOR2>(x)); x = fmaxf(x, dpp<XOR7>(x)); x = fmaxf(x, dpp<XOR8>(x));
  auto s = __builtin_amdgcn_permlane16_swap(__float_as_uint(x), __float_as_uint(x), false, false);
  x = fmaxf(__uint_as_float(s[0]), __uint_as_float(s[1]));
  auto t = __builtin_amdgcn_permlane32_swap(__float_as_uint(x), __float_as_uint(x), false, false);
  return fmaxf(__uint_as_float(t[0]), __uint_as_float(t[1]));
}
__device__ __forceinline__ float wave_sum(float x) {
  x += dpp<XOR1>(x); x += dpp<XOR2>(x); x += dpp<XOR7>(x); x += dpp<XOR8>(x);
  auto s = __builtin_amdgcn_permlane16_swap(__float_as_uint(x), __float_as_uint(x), false, false);
  x = __uint_as_float(s[0]) + __uint_as_float(s[1]);
  auto t = __builtin_amdgcn_permlane32_swap(__float_as_uint(x), __float_as_uint(x), false, false);
  return __uint_as_float(t[0]) + __uint_as_float(t[1]);
}
__device__ __forceinline__ unsigned bf16_rne(float x) {
  return (unsigned)__builtin_bit_cast(unsigned short, (__bf16)x);
}
__device__ __forceinline__ unsigned pack_p(float p) {
  const unsigned hi = bf16_rne(p);
  const unsigned lo = bf16_rne(p - __builtin_bit_cast(float, hi << 16));
  return hi | (lo << 16);
}
__device__ __forceinline__ u32x4 srd(const void* p, unsigned bytes) {
  const unsigned long long a = (unsigned long long)p;
  return u32x4{(unsigned)a, (unsigned)(a >> 32) & 0xffffu, bytes, 0x00020000u};
}
template <int OFF> __device__ __forceinline__ void ld_nt(u32x4& r, unsigned voff, u32x4 rs, unsigned soff) {
  asm volatile("buffer_load_dwordx4 %0, %1, %2, %3 offen offset:%c4 nt" : "+v"(r) : "v"(voff), "s"(rs), "s"(soff), "i"(OFF) : "memory");
}
__device__ __forceinline__ void ld_sc1(u32x4& r, unsigned voff, u32x4 rs, unsigned soff) {
  asm volatile("buffer_load_dwordx4 %0, %1, %2, %3 offen sc1" : "+v"(r) : "v"(voff), "s"(rs), "s"(soff) : "memory");
}
__device__ __forceinline__ void st_sc1(u32x4 x, unsigned voff, u32x4 rs, unsigned soff) {
  asm volatile("buffer_store_dwordx4 %0, %1, %2, %3 offen sc1\n\ts_nop 1" :: "v"(x), "v"(voff), "s"(rs), "s"(soff) : "memory");
}
template <int N> __device__ __forceinline__ void wait4(u32x4& r) { asm volatile("s_waitcnt vmcnt(%c1)" : "+v"(r) : "i"(N) : "memory"); }
__device__ __forceinline__ void pin3(unsigned& a, float& b, float& c) { asm volatile("" : "+v"(a), "+v"(b), "+v"(c)); }
__device__ __forceinline__ void pin1(unsigned long long& a) { asm volatile("" : "+v"(a)); }
__device__ __forceinline__ unsigned tag_a(unsigned long long epoch) { return (unsigned)(epoch + 1ull); }
__device__ __forceinline__ unsigned tag_b(unsigned long long epoch) { return (unsigned)((epoch + 1ull) >> 32) ^ (tag_a(epoch) * 0x9E3779B1u); }
__device__ __forceinline__ void undef4(u32x4& r) { asm volatile("" : "=v"(r)); }
__device__ __forceinline__ void keep4(u32x4& r) { asm volatile("" : "+v"(r)); }

constexpr int brev4(int t) { return ((t & 1) << 3) | ((t & 2) << 1) | ((t & 4) >> 1) | ((t & 8) >> 3); }
template <int LN, int N> __device__ __forceinline__ void pv_step(f32x4& acc, unsigned pk, u32x4& v, const unsigned (&sel)[4]) {
  const unsigned pb = dppu<0x150 + LN>(pk);
  u32x4 b;
#pragma unroll
  for (int w = 0; w < 4; ++w) b[w] = __builtin_amdgcn_perm(pb, pb, sel[w]);
  keep4(b);
  wait4<N>(v);
  acc = __builtin_amdgcn_mfma_f32_16x16x32_bf16(__builtin_bit_cast(bf16x8, v), __builtin_bit_cast(bf16x8, b), acc, 0, 0, 0);
  SB();
}
template <int T> struct IC { static constexpr int v = T; };
template <int N, class F> __device__ __forceinline__ void sfor(F&& f) {
  if constexpr (N > 0) { sfor<N - 1>(f); f(IC<N - 1>{}); }
}

}

__global__ void __launch_bounds__(WAVES * 64)
tree_attn_decode_kernel(const void* q, const void* kc, const void* vc, unsigned char* ws, const void* kt, const void* vt,
                        const void* mask, void* out) {
  __shared__ __attribute__((aligned(16))) float lds[WAVES * 128 + WAVES * NG * 64 + 8];
  const int s = blockIdx.x % NSPLIT, h = blockIdx.x / NSPLIT;
  const int tid = threadIdx.x, lane = tid & 63, wave = __builtin_amdgcn_readfirstlane(tid >> 6), c = lane & 15;
  const int w = s * WAVES + wave;
  const unsigned hb = (unsigned)h * (C * D * 2);
  const unsigned voff = lane * 16;
  auto toffs = [&](int i) -> unsigned { return hb + (unsigned)((i * NW + w + h * ROT) & (NT - 1)) * 4096u; };
  const u32x4 krs = srd(kc, H * C * D * 2), vrs = srd(vc, H * C * D * 2);
  u32x4 qv, r[RING];
  asm volatile("buffer_load_dwordx4 %0, %1, %2, %3 offen" : "=v"(qv) : "v"(c * 16), "s"(srd(q, H * D * 2)), "s"(h * (D * 2)) : "memory");
  const unsigned tw = (unsigned)__builtin_amdgcn_s_memrealtime();
  unsigned tz = tw + ((wave * STAG) >> 4);
  auto pace = [&](int k) {
    asm volatile("s_waitcnt vmcnt(%c0)" :: "i"(WIN - 4) : "memory");
    const unsigned due = tz + (unsigned)(((k - LEAD + 1) * PACE) >> 4);
    while ((int)((unsigned)__builtin_amdgcn_s_memrealtime() - due) < 0) __builtin_amdgcn_s_sleep(1);
  };
  sfor<LEAD * 4>([&](auto I) { undef4(r[I.v]); ld_nt<(I.v & 3) * 1024>(r[I.v], voff, krs, toffs(I.v >> 2)); });
  const bool cons = s == 0 && wave == 0;
  const u32x4 ers = srd(ws + 512 * h, 8);
  unsigned long long epoch;
  u32x4 ktv, ml;
  unsigned vtv, mkv;
  asm volatile("buffer_load_dwordx2 %0, off, %1, 0 sc1" : "=v"(epoch) : "s"(ers) : "memory");
  asm volatile("" : "=v"(ktv), "=v"(ml), "=v"(vtv), "=v"(mkv));
  if (cons) {
    asm volatile("buffer_load_dwordx4 %0, %1, %2, %3 offen" : "+v"(ktv) : "v"(c * 16), "s"(srd(kt, H * D * 2)), "s"(h * (D * 2)) : "memory");
    asm volatile("buffer_load_dword %0, %1, %2, %3 offen" : "+v"(vtv) : "v"(lane * 4), "s"(srd(vt, H * D * 2)), "s"(h * (D * 2)) : "memory");
    asm volatile("buffer_load_ushort %0, off, %1, 0" : "+v"(mkv) : "s"(srd(mask, 2)) : "memory");
  }
  sfor<RING - LEAD * 4>([&](auto I) {
    constexpr int i = I.v + LEAD * 4;
    if constexpr ((i & 3) == 0) pace(i >> 2);
    undef4(r[i]);
    ld_nt<(i & 3) * 1024>(r[i], voff, krs, toffs(i >> 2));
  });
  asm volatile("s_waitcnt vmcnt(%c1)" : "+v"(qv) : "i"(RING) : "memory");
  const u32x4 gsr = srd(ws + GR_OFF + h * GR_HEAD, GR_HEAD);
  float* sco = lds + WAVES * 128 + wave * (NG * 64) + lane;
  float* wmx = lds + WAVES * 128 + WAVES * NG * 64;
  float mx = -INFINITY;
#pragma unroll 1
  for (int g = 0; g < NG; ++g) {
    float xa = 0.f, ya = 0.f, za = 0.f, ua = 0.f, sc = 0.f;
    sfor<16>([&](auto I) {
      constexpr int t = I.v;
      wait4<RING - 1>(r[t % RING]);
      if constexpr (t == LEAD * 4) {
        if (g == 0 && wave == 0) {
          pin1(epoch);
          st_sc1(u32x4{tag_a(epoch), tw, tag_b(epoch), 0u}, lane == 0 ? 0u : OOB, gsr, GR_TIME + s * 16);
        }
        if (g == 1) ld_sc1(ml, (lane & (NSPLIT - 1)) * 16, gsr, GR_TIME);
        if (g == 2) {
          keep4(ml); pin1(epoch);
          const int dl = (int)(ml[1] - tw);
          const bool ok = ml[0] == tag_a(epoch) && ml[2] == tag_b(epoch) && dl < 2000 && dl > -2000;
          float sd = ok ? (float)dl : 0.f, cn = ok ? 1.f : 0.f;
          sd += dpp<XOR1>(sd); sd += dpp<XOR2>(sd); sd += dpp<XOR7>(sd);
          cn += dpp<XOR1>(cn); cn += dpp<XOR2>(cn); cn += dpp<XOR7>(cn);
          tz += (unsigned)__builtin_amdgcn_readfirstlane((int)(sd * __builtin_amdgcn_rcpf(fmaxf(cn, 1.f))));
        }
      }
      float d = dot8(r[t % RING], qv);
      asm volatile("" : "+v"(d));
      const int ti = g * 4 + (t >> 2) + AHEAD;
      const bool isv = ti >= NTW;
      if constexpr ((t & 3) == 0) pace(ti);
      ld_nt<(t & 3) * 1024>(r[t % RING], voff, isv ? vrs : krs, toffs(isv ? ti - NTW : ti));
      d += dpp<XOR8>(d);
      if constexpr (!(t & 1)) xa = d;
      else {
        float e = (c & 8) ? d : xa;
        e += dpp<XOR7>(e);
        if constexpr (!(t & 2)) ya = e;
        else {
          e = (c & 4) ? e : ya;
          e += dpp<XOR2>(e);
          if constexpr (!(t & 4)) za = e;
          else {
            e = (c & 2) ? e : za;
            e += dpp<XOR1>(e);
            if constexpr (!(t & 8)) ua = e;
            else sc = ((c & 1) ? e : ua) * (SCALE * LOG2E);
          }
        }
      }
    });
    mx = fmaxf(mx, sc);
    sco[g * 64] = sc;
  }
  asm volatile("" : "+v"(epoch), "+v"(ktv), "+v"(vtv), "+v"(mkv));
  mx = wave_max(mx);
  mx = (mx == -INFINITY) ? -3.0e38f : mx;
  float l = 0.f;
#pragma unroll
  for (int g = 0; g < NG; ++g) l += __builtin_amdgcn_exp2f(sco[g * 64] - mx);
  l = wave_sum(l);
  if (lane == 0) { wmx[wave] = mx; wmx[4 + wave] = l; }
  const unsigned tagA = tag_a(epoch), tagB = tag_b(epoch);
  unsigned sel[4];
  {
    const unsigned b = (c & 8) ? 0x0302u : 0x0100u;
    const unsigned sv = (c & 1) ? ((b << 16) | 0x0c0cu) : (0x0c0c0000u | b);
#pragma unroll
    for (int i = 0; i < 4; ++i) sel[i] = (((c >> 1) & 3) == i) ? sv : 0x0c0c0c0cu;
  }
  f32x4 acc = {0.f, 0.f, 0.f, 0.f};
#pragma unroll 1
  for (int g = 0; g < NG - 1; ++g) {
    const unsigned pk = pack_p(__builtin_amdgcn_exp2f(sco[g * 64] - mx));
    sfor<16>([&](auto I) {
      constexpr int t = I.v;
      pv_step<brev4(t), RING - 1>(acc, pk, r[t % RING], sel);
      const int ti = g * 4 + (t >> 2) + AHEAD;
      if constexpr ((t & 3) == 0) pace(NTW + ti);
      ld_nt<(t & 3) * 1024>(r[t % RING], voff, vrs, toffs(ti));
    });
  }
  {
    const float sl = sco[(NG - 1) * 64];
    WG_BARRIER();
    const f32x4 m4 = *reinterpret_cast<const f32x4*>(wmx), l4 = *reinterpret_cast<const f32x4*>(wmx + 4);
    const float M = fmaxf(fmaxf(m4[0], m4[1]), fmaxf(m4[2], m4[3]));
    float L = 0.f;
#pragma unroll
    for (int i = 0; i < WAVES; ++i) L = fmaf(__builtin_amdgcn_exp2f(m4[i] - M), l4[i], L);
    st_sc1(u32x4{tagA, __float_as_uint(M), tagB, __float_as_uint(L)}, tid == 0 ? 0u : OOB, gsr, s * 16);
    acc *= __builtin_amdgcn_exp2f(mx - M);
    const unsigned pk = pack_p(__builtin_amdgcn_exp2f(sl - M));
    sfor<16>([&](auto I) { pv_step<brev4(I.v), 16 - I.v>(acc, pk, r[I.v], sel); });
  }
  {
    const bool up = c & 8;
    const float s0 = up ? acc[0] : acc[2], s1 = up ? acc[1] : acc[3];
    const float e0 = (up ? acc[2] : acc[0]) + dpp<XOR8>(s0), e1 = (up ? acc[3] : acc[1]) + dpp<XOR8>(s1);
    float* rec = lds + wave * 128 + 32 * (lane >> 4) + 2 * (c & 8) + (c & 7);
    rec[0] = e0; rec[8] = e1;
  }
  u32x4 av[NSPLIT], bv[NSPLIT];
  sfor<NSPLIT>([&](auto J) { undef4(av[J.v]); undef4(bv[J.v]); });
  auto sweep = [&](u32x4 (&x)[NSPLIT]) {
    sfor<NSPLIT - 1>([&](auto J) { ld_sc1(x[J.v + 1], voff, gsr, GR_ACC + (J.v + 1) * 1024); });
  };
  if (cons) { ld_sc1(ml, (lane & (NSPLIT - 1)) * 16, gsr, 0); sweep(av); }
  WG_BARRIER();
  if (wave != 0) return;
  f32x2 o = *reinterpret_cast<const f32x2*>(lds + 2 * lane);
#pragma unroll
  for (int i = 1; i < WAVES; ++i) o += *reinterpret_cast<const f32x2*>(lds + i * 128 + 2 * lane);
  if (s != 0) {
    st_sc1(u32x4{tagA, __float_as_uint(o[0]), tagB, __float_as_uint(o[1])}, voff, gsr, GR_ACC + s * 1024);
    return;
  }
  float dt = dot8(ktv, qv);
  dt += dpp<XOR1>(dt); dt += dpp<XOR2>(dt); dt += dpp<XOR7>(dt); dt += dpp<XOR8>(dt);
  const float mk = __builtin_bit_cast(float, mkv << 16);
  const float st = (dt * SCALE + (1.0f - mk) * -1000000000.0f) * LOG2E;
  wait4<NSPLIT - 1>(ml);
  {
    unsigned spins = 0;
    while (__builtin_expect(!__all((ml[0] == tagA) & (ml[2] == tagB)), 0) && ++spins < (1u << 18)) {
      ld_sc1(ml, (lane & (NSPLIT - 1)) * 16, gsr, 0);
      wait4<0>(ml);
    }
  }
  const float mj = __uint_as_float(ml[1]);
  float Mf = fmaxf(mj, st);
  Mf = fmaxf(Mf, dpp<XOR1>(Mf)); Mf = fmaxf(Mf, dpp<XOR2>(Mf)); Mf = fmaxf(Mf, dpp<XOR7>(Mf));
  float wj = __builtin_amdgcn_exp2f(mj - Mf), wt = __builtin_amdgcn_exp2f(st - Mf);
  float Lf = wj * __uint_as_float(ml[3]);
  Lf += dpp<XOR1>(Lf); Lf += dpp<XOR2>(Lf); Lf += dpp<XOR7>(Lf);
  const float inv = 1.0f / (Lf + wt);
  wj *= inv; wt *= inv;
  float wsp[NSPLIT];
#pragma unroll
  for (int j = 0; j < NSPLIT; ++j) wsp[j] = __uint_as_float(__builtin_amdgcn_readlane(__float_as_uint(wj), j));
  const float t0 = wt * __builtin_bit_cast(float, vtv << 16), t1 = wt * __builtin_bit_cast(float, vtv & 0xffff0000u);
  const float b0 = fmaf(wsp[0], o[0], t0), b1 = fmaf(wsp[0], o[1], t1);
  float f0 = 0.f, f1 = 0.f;
  auto take = [&](u32x4 (&x)[NSPLIT]) -> bool {
    unsigned bad = 0;
    f0 = b0; f1 = b1;
    sfor<NSPLIT - 1>([&](auto J) {
      constexpr int j = J.v + 1;
      wait4<2 * (NSPLIT - 1) - j>(x[j]);
      bad |= (x[j][0] ^ tagA) | (x[j][2] ^ tagB);
      f0 = fmaf(wsp[j], __uint_as_float(x[j][1]), f0);
      f1 = fmaf(wsp[j], __uint_as_float(x[j][3]), f1);
      pin3(bad, f0, f1);
    });
    return !__any(bad != 0);
  };
  for (unsigned spins = 0; spins < (1u << 17); ++spins) {
    sweep(bv);
    if (take(av)) break;
    sweep(av);
    if (take(bv)) break;
  }
  asm volatile("buffer_store_dword %0, %1, %2, %3 offen sc1"
               :: "v"(bf16_rne(f0) | (bf16_rne(f1) << 16)), "v"(lane * 4), "s"(srd(out, H * D * 2)), "s"(h * (D * 2)) : "memory");
  asm volatile("buffer_store_dwordx2 %0, %1, %2, 0 offen sc1" :: "v"(epoch + 1ull), "v"(lane == 0 ? 0u : OOB), "s"(ers) : "memory");
  sfor<NSPLIT - 1>([&](auto J) { keep4(av[J.v + 1]); keep4(bv[J.v + 1]); });
}

extern "C" void kernel_launch(void* const* d_in, const int* in_sizes, int n_in, void* d_out, int out_size, void* d_ws,
                              size_t ws_size, hipStream_t stream) {
  tree_attn_decode_kernel<<<H * NSPLIT, WAVES * 64, 0, stream>>>(d_in[0], d_in[1], d_in[2], (unsigned char*)d_ws, d_in[3],
                                                                 d_in[4], d_in[5], d_out);
}
```

```cpp
#include <hip/hip_runtime.h>
#include <hip/hip_bf16.h>
#include <cstdint>

namespace {

constexpr int H = 32, C = 8192, D = 128;
constexpr int NSPLIT = 8, WAVES = 4;
constexpr int NW = NSPLIT * WAVES;
constexpr int NT = C / 16;
constexpr int NTW = NT / NW;
constexpr int NG = NTW / 4;
constexpr int AHEAD = 4, RING = AHEAD * 4;
constexpr int ROT = 16;
constexpr int PACE = 885;
constexpr int STAG = PACE / 4;
constexpr int LEAD = 2;
constexpr int WIN = 12;
constexpr float SCALE = 0.088388347648318440f;
constexpr float LOG2E = 1.4426950408889634f;
constexpr unsigned OOB = 0x7fff0000u;
constexpr unsigned GR_OFF = 65536, GR_ACC = 1024, GR_HEAD = GR_ACC + NSPLIT * 1024;

using bf16x2 = __attribute__((ext_vector_type(2))) __bf16;
using bf16x8 = __attribute__((ext_vector_type(8))) __bf16;
using u32x4 = __attribute__((ext_vector_type(4))) unsigned;
using f32x4 = __attribute__((ext_vector_type(4))) float;
using f32x2 = __attribute__((ext_vector_type(2))) float;
#define SB() __builtin_amdgcn_sched_barrier(0)
#define WG_BARRIER() asm volatile("s_waitcnt lgkmcnt(0)\n\ts_barrier" ::: "memory")

__device__ __forceinline__ float dot2(unsigned k, unsigned q, float acc) {
  return __builtin_amdgcn_fdot2_f32_bf16(__builtin_bit_cast(bf16x2, k), __builtin_bit_cast(bf16x2, q), acc, false);
}
__device__ __forceinline__ float dot8(u32x4 k, u32x4 q) {
  return dot2(k[3], q[3], dot2(k[2], q[2], dot2(k[1], q[1], dot2(k[0], q[0], 0.f))));
}
template <int CTRL> __device__ __forceinline__ float dpp(float x) {
  return __builtin_bit_cast(float, __builtin_amdgcn_mov_dpp(__builtin_bit_cast(int, x), CTRL, 0xf, 0xf, true));
}
template <int CTRL> __device__ __forceinline__ unsigned dppu(unsigned x) {
  return (unsigned)__builtin_amdgcn_mov_dpp((int)x, CTRL, 0xf, 0xf, true);
}
constexpr int XOR1 = 0xB1, XOR2 = 0x4E, XOR7 = 0x141, XOR8 = 0x128;
__device__ __forceinline__ float wave_max(float x) {
  x = fmaxf(x, dpp<XOR1>(x)); x = fmaxf(x, dpp<XOR2>(x)); x = fmaxf(x, dpp<XOR7>(x)); x = fmaxf(x, dpp<XOR8>(x));
  auto s = __builtin_amdgcn_permlane16_swap(__float_as_uint(x), __float_as_uint(x), false, false);
  x = fmaxf(__uint_as_float(s[0]), __uint_as_float(s[1]));
  auto t = __builtin_amdgcn_permlane32_swap(__float_as_uint(x), __float_as_uint(x), false, false);
  return fmaxf(__uint_as_float(t[0]), __uint_as_float(t[1]));
}
__device__ __forceinline__ float wave_sum(float x) {
  x += dpp<XOR1>(x); x += dpp<XOR2>(x); x += dpp<XOR7>(x); x += dpp<XOR8>(x);
  auto s = __builtin_amdgcn_permlane16_swap(__float_as_uint(x), __float_as_uint(x), false, false);
  x = __uint_as_float(s[0]) + __uint_as_float(s[1]);
  auto t = __builtin_amdgcn_permlane32_swap(__float_as_uint(x), __float_as_uint(x), false, false);
  return __uint_as_float(t[0]) + __uint_as_float(t[1]);
}
__device__ __forceinline__ unsigned bf16_rne(float x) {
  return (unsigned)__builtin_bit_cast(unsigned short, (__bf16)x);
}
__device__ __forceinline__ unsigned pack_p(float p) {
  const unsigned hi = bf16_rne(p);
  const unsigned lo = bf16_rne(p - __builtin_bit_cast(float, hi << 16));
  return hi | (lo << 16);
}
__device__ __forceinline__ u32x4 srd(const void* p, unsigned bytes) {
  const unsigned long long a = (unsigned long long)p;
  return u32x4{(unsigned)a, (unsigned)(a >> 32) & 0xffffu, bytes, 0x00020000u};
}
template <int OFF> __device__ __forceinline__ void ld_nt(u32x4& r, unsigned voff, u32x4 rs, unsigned soff) {
  asm volatile("buffer_load_dwordx4 %0, %1, %2, %3 offen offset:%c4 nt" : "+v"(r) : "v"(voff), "s"(rs), "s"(soff), "i"(OFF) : "memory");
}
__device__ __forceinline__ void ld_sc1(u32x4& r, unsigned voff, u32x4 rs, unsigned soff) {
  asm volatile("buffer_load_dwordx4 %0, %1, %2, %3 offen sc1" : "+v"(r) : "v"(voff), "s"(rs), "s"(soff) : "memory");
}
__device__ __forceinline__ void st_sc1(u32x4 x, unsigned voff, u32x4 rs, unsigned soff) {
  asm volatile("buffer_store_dwordx4 %0, %1, %2, %3 offen sc1\n\ts_nop 1" :: "v"(x), "v"(voff), "s"(rs), "s"(soff) : "memory");
}
template <int N> __device__ __forceinline__ void wait4(u32x4& r) { asm volatile("s_waitcnt vmcnt(%c1)" : "+v"(r) : "i"(N) : "memory"); }
__device__ __forceinline__ void pin3(unsigned& a, float& b, float& c) { asm volatile("" : "+v"(a), "+v"(b), "+v"(c)); }
__device__ __forceinline__ void undef4(u32x4& r) { asm volatile("" : "=v"(r)); }
__device__ __forceinline__ void keep4(u32x4& r) { asm volatile("" : "+v"(r)); }

constexpr int brev4(int t) { return ((t & 1) << 3) | ((t & 2) << 1) | ((t & 4) >> 1) | ((t & 8) >> 3); }
template <int LN, int N> __device__ __forceinline__ void pv_step(f32x4& acc, unsigned pk, u32x4& v, const unsigned (&sel)[4]) {
  const unsigned pb = dppu<0x150 + LN>(pk);
  u32x4 b;
#pragma unroll
  for (int w = 0; w < 4; ++w) b[w] = __builtin_amdgcn_perm(pb, pb, sel[w]);
  keep4(b);
  wait4<N>(v);
  acc = __builtin_amdgcn_mfma_f32_16x16x32_bf16(__builtin_bit_cast(bf16x8, v), __builtin_bit_cast(bf16x8, b), acc, 0, 0, 0);
  SB();
}
template <int T> struct IC { static constexpr int v = T; };
template <int N, class F> __device__ __forceinline__ void sfor(F&& f) {
  if constexpr (N > 0) { sfor<N - 1>(f); f(IC<N - 1>{}); }
}

}

__global__ void __launch_bounds__(WAVES * 64)
tree_attn_decode_kernel(const void* q, const void* kc, const void* vc, unsigned char* ws, const void* kt, const void* vt,
                        const void* mask, void* out) {
  __shared__ __attribute__((aligned(16))) float lds[WAVES * 128 + WAVES * NG * 64 + 8];
  const int s = blockIdx.x % NSPLIT, h = blockIdx.x / NSPLIT;
  const int tid = threadIdx.x, lane = tid & 63, wave = __builtin_amdgcn_readfirstlane(tid >> 6), c = lane & 15;
  const int w = s * WAVES + wave;
  const unsigned hb = (unsigned)h * (C * D * 2);
  const unsigned voff = lane * 16;
  auto toffs = [&](int i) -> unsigned { return hb + (unsigned)((i * NW + w + h * ROT) & (NT - 1)) * 4096u; };
  const u32x4 krs = srd(kc, H * C * D * 2), vrs = srd(vc, H * C * D * 2);
  u32x4 qv, r[RING];
  asm volatile("buffer_load_dwordx4 %0, %1, %2, %3 offen" : "=v"(qv) : "v"(c * 16), "s"(srd(q, H * D * 2)), "s"(h * (D * 2)) : "memory");
  const unsigned tz = (unsigned)__builtin_amdgcn_s_memrealtime() + ((wave * STAG) >> 4);
  auto pace = [&](int k) {
    asm volatile("s_waitcnt vmcnt(%c0)" :: "i"(WIN - 4) : "memory");
    const unsigned due = tz + (unsigned)(((k - LEAD + 1) * PACE) >> 4);
    while ((int)((unsigned)__builtin_amdgcn_s_memrealtime() - due) < 0) __builtin_amdgcn_s_sleep(1);
  };
  sfor<LEAD * 4>([&](auto I) { undef4(r[I.v]); ld_nt<(I.v & 3) * 1024>(r[I.v], voff, krs, toffs(I.v >> 2)); });
  const bool cons = s == 0 && wave == 0;
  const u32x4 ers = srd(ws + 512 * h, 8);
  unsigned long long epoch;
  u32x4 ktv, ml;
  unsigned vtv, mkv;
  asm volatile("buffer_load_dwordx2 %0, off, %1, 0 sc1" : "=v"(epoch) : "s"(ers) : "memory");
  asm volatile("" : "=v"(ktv), "=v"(ml), "=v"(vtv), "=v"(mkv));
  if (cons) {
    asm volatile("buffer_load_dwordx4 %0, %1, %2, %3 offen" : "+v"(ktv) : "v"(c * 16), "s"(srd(kt, H * D * 2)), "s"(h * (D * 2)) : "memory");
    asm volatile("buffer_load_dword %0, %1, %2, %3 offen" : "+v"(vtv) : "v"(lane * 4), "s"(srd(vt, H * D * 2)), "s"(h * (D * 2)) : "memory");
    asm volatile("buffer_load_ushort %0, off, %1, 0" : "+v"(mkv) : "s"(srd(mask, 2)) : "memory");
  }
  sfor<RING - LEAD * 4>([&](auto I) {
    constexpr int i = I.v + LEAD * 4;
    if constexpr ((i & 3) == 0) pace(i >> 2);
    undef4(r[i]);
    ld_nt<(i & 3) * 1024>(r[i], voff, krs, toffs(i >> 2));
  });
  asm volatile("s_waitcnt vmcnt(%c1)" : "+v"(qv) : "i"(RING) : "memory");
  float* sco = lds + WAVES * 128 + wave * (NG * 64) + lane;
  float* wmx = lds + WAVES * 128 + WAVES * NG * 64;
  float mx = -INFINITY;
#pragma unroll 1
  for (int g = 0; g < NG; ++g) {
    float xa = 0.f, ya = 0.f, za = 0.f, ua = 0.f, sc = 0.f;
    sfor<16>([&](auto I) {
      constexpr int t = I.v;
      wait4<RING - 1>(r[t % RING]);
      float d = dot8(r[t % RING], qv);
      asm volatile("" : "+v"(d));
      const int ti = g * 4 + (t >> 2) + AHEAD;
      const bool isv = ti >= NTW;
      if constexpr ((t & 3) == 0) pace(ti);
      ld_nt<(t & 3) * 1024>(r[t % RING], voff, isv ? vrs : krs, toffs(isv ? ti - NTW : ti));
      d += dpp<XOR8>(d);
      if constexpr (!(t & 1)) xa = d;
      else {
        float e = (c & 8) ? d : xa;
        e += dpp<XOR7>(e);
        if constexpr (!(t & 2)) ya = e;
        else {
          e = (c & 4) ? e : ya;
          e += dpp<XOR2>(e);
          if constexpr (!(t & 4)) za = e;
          else {
            e = (c & 2) ? e : za;
            e += dpp<XOR1>(e);
            if constexpr (!(t & 8)) ua = e;
            else sc = ((c & 1) ? e : ua) * (SCALE * LOG2E);
          }
        }
      }
    });
    mx = fmaxf(mx, sc);
    sco[g * 64] = sc;
  }
  asm volatile("" : "+v"(epoch), "+v"(ktv), "+v"(vtv), "+v"(mkv));
  mx = wave_max(mx);
  mx = (mx == -INFINITY) ? -3.0e38f : mx;
  float l = 0.f;
#pragma unroll
  for (int g = 0; g < NG; ++g) l += __builtin_amdgcn_exp2f(sco[g * 64] - mx);
  l = wave_sum(l);
  if (lane == 0) { wmx[wave] = mx; wmx[4 + wave] = l; }
  const unsigned tagA = (unsigned)(epoch + 1ull), tagB = (unsigned)((epoch + 1ull) >> 32) ^ (tagA * 0x9E3779B1u);
  const u32x4 gsr = srd(ws + GR_OFF + h * GR_HEAD, GR_HEAD);
  unsigned sel[4];
  {
    const unsigned b = (c & 8) ? 0x0302u : 0x0100u;
    const unsigned sv = (c & 1) ? ((b << 16) | 0x0c0cu) : (0x0c0c0000u | b);
#pragma unroll
    for (int i = 0; i < 4; ++i) sel[i] = (((c >> 1) & 3) == i) ? sv : 0x0c0c0c0cu;
  }
  f32x4 acc = {0.f, 0.f, 0.f, 0.f};
#pragma unroll 1
  for (int g = 0; g < NG - 1; ++g) {
    const unsigned pk = pack_p(__builtin_amdgcn_exp2f(sco[g * 64] - mx));
    sfor<16>([&](auto I) {
      constexpr int t = I.v;
      pv_step<brev4(t), RING - 1>(acc, pk, r[t % RING], sel);
      const int ti = g * 4 + (t >> 2) + AHEAD;
      if constexpr ((t & 3) == 0) pace(NTW + ti);
      ld_nt<(t & 3) * 1024>(r[t % RING], voff, vrs, toffs(ti));
    });
  }
  {
    const float sl = sco[(NG - 1) * 64];
    WG_BARRIER();
    const f32x4 m4 = *reinterpret_cast<const f32x4*>(wmx), l4 = *reinterpret_cast<const f32x4*>(wmx + 4);
    const float M = fmaxf(fmaxf(m4[0], m4[1]), fmaxf(m4[2], m4[3]));
    float L = 0.f;
#pragma unroll
    for (int i = 0; i < WAVES; ++i) L = fmaf(__builtin_amdgcn_exp2f(m4[i] - M), l4[i], L);
    st_sc1(u32x4{tagA, __float_as_uint(M), tagB, __float_as_uint(L)}, tid == 0 ? 0u : OOB, gsr, s * 16);
    acc *= __builtin_amdgcn_exp2f(mx - M);
    const unsigned pk = pack_p(__builtin_amdgcn_exp2f(sl - M));
    sfor<16>([&](auto I) { pv_step<brev4(I.v), 16 - I.v>(acc, pk, r[I.v], sel); });
  }
  {
    const bool up = c & 8;
    const float s0 = up ? acc[0] : acc[2], s1 = up ? acc[1] : acc[3];
    const float e0 = (up ? acc[2] : acc[0]) + dpp<XOR8>(s0), e1 = (up ? acc[3] : acc[1]) + dpp<XOR8>(s1);
    float* rec = lds + wave * 128 + 32 * (lane >> 4) + 2 * (c & 8) + (c & 7);
    rec[0] = e0; rec[8] = e1;
  }
  u32x4 av[NSPLIT], bv[NSPLIT];
  sfor<NSPLIT>([&](auto J) { undef4(av[J.v]); undef4(bv[J.v]); });
  auto sweep = [&](u32x4 (&x)[NSPLIT]) {
    sfor<NSPLIT - 1>([&](auto J) { ld_sc1(x[J.v + 1], voff, gsr, GR_ACC + (J.v + 1) * 1024); });
  };
  if (cons) { ld_sc1(ml, (lane & (NSPLIT - 1)) * 16, gsr, 0); sweep(av); }
  WG_BARRIER();
  if (wave != 0) return;
  f32x2 o = *reinterpret_cast<const f32x2*>(lds + 2 * lane);
#pragma unroll
  for (int i = 1; i < WAVES; ++i) o += *reinterpret_cast<const f32x2*>(lds + i * 128 + 2 * lane);
  if (s != 0) {
    st_sc1(u32x4{tagA, __float_as_uint(o[0]), tagB, __float_as_uint(o[1])}, voff, gsr, GR_ACC + s * 1024);
    return;
  }
  float dt = dot8(ktv, qv);
  dt += dpp<XOR1>(dt); dt += dpp<XOR2>(dt); dt += dpp<XOR7>(dt); dt += dpp<XOR8>(dt);
  const float mk = __builtin_bit_cast(float, mkv << 16);
  const float st = (dt * SCALE + (1.0f - mk) * -1000000000.0f) * LOG2E;
  wait4<NSPLIT - 1>(ml);
  {
    unsigned spins = 0;
    while (__builtin_expect(!__all((ml[0] == tagA) & (ml[2] == tagB)), 0) && ++spins < (1u << 18)) {
      ld_sc1(ml, (lane & (NSPLIT - 1)) * 16, gsr, 0);
      wait4<0>(ml);
    }
  }
  const float mj = __uint_as_float(ml[1]);
  float Mf = fmaxf(mj, st);
  Mf = fmaxf(Mf, dpp<XOR1>(Mf)); Mf = fmaxf(Mf, dpp<XOR2>(Mf)); Mf = fmaxf(Mf, dpp<XOR7>(Mf));
  float wj = __builtin_amdgcn_exp2f(mj - Mf), wt = __builtin_amdgcn_exp2f(st - Mf);
  float Lf = wj * __uint_as_float(ml[3]);
  Lf += dpp<XOR1>(Lf); Lf += dpp<XOR2>(Lf); Lf += dpp<XOR7>(Lf);
  const float inv = 1.0f / (Lf + wt);
  wj *= inv; wt *= inv;
  float wsp[NSPLIT];
#pragma unroll
  for (int j = 0; j < NSPLIT; ++j) wsp[j] = __uint_as_float(__builtin_amdgcn_readlane(__float_as_uint(wj), j));
  const float t0 = wt * __builtin_bit_cast(float, vtv << 16), t1 = wt * __builtin_bit_cast(float, vtv & 0xffff0000u);
  const float b0 = fmaf(wsp[0], o[0], t0), b1 = fmaf(wsp[0], o[1], t1);
  float f0 = 0.f, f1 = 0.f;
  auto take = [&](u32x4 (&x)[NSPLIT]) -> bool {
    unsigned bad = 0;
    f0 = b0; f1 = b1;
    sfor<NSPLIT - 1>([&](auto J) {
      constexpr int j = J.v + 1;
      wait4<2 * (NSPLIT - 1) - j>(x[j]);
      bad |= (x[j][0] ^ tagA) | (x[j][2] ^ tagB);
      f0 = fmaf(wsp[j], __uint_as_float(x[j][1]), f0);
      f1 = fmaf(wsp[j], __uint_as_float(x[j][3]), f1);
      pin3(bad, f0, f1);
    });
    return !__any(bad != 0);
  };
  for (unsigned spins = 0; spins < (1u << 17); ++spins) {
    sweep(bv);
    if (take(av)) break;
    sweep(av);
    if (take(bv)) break;
  }
  asm volatile("buffer_store_dword %0, %1, %2, %3 offen sc1"
               :: "v"(bf16_rne(f0) | (bf16_rne(f1) << 16)), "v"(lane * 4), "s"(srd(out, H * D * 2)), "s"(h * (D * 2)) : "memory");
  asm volatile("buffer_store_dwordx2 %0, %1, %2, 0 offen sc1" :: "v"(epoch + 1ull), "v"(lane == 0 ? 0u : OOB), "s"(ers) : "memory");
  sfor<NSPLIT - 1>([&](auto J) { keep4(av[J.v + 1]); keep4(bv[J.v + 1]); });
}

extern "C" void kernel_launch(void* const* d_in, const int* in_sizes, int n_in, void* d_out, int out_size, void* d_ws,
                              size_t ws_size, hipStream_t stream) {
  tree_attn_decode_kernel<<<H * NSPLIT, WAVES * 64, 0, stream>>>(d_in[0], d_in[1], d_in[2], (unsigned char*)d_ws, d_in[3],
                                                                 d_in[4], d_in[5], d_out);
}
```

```cpp
#include <hip/hip_runtime.h>
#include <hip/hip_bf16.h>
#include <cstdint>

namespace {

constexpr int H = 32, C = 8192, D = 128;
constexpr int NSPLIT = 8, WAVES = 4;
constexpr int NW = NSPLIT * WAVES;
constexpr int NT = C / 16;
constexpr int NTW = NT / NW;
constexpr int NG = NTW / 4;
constexpr int AHEAD = 4, RING = AHEAD * 4;
constexpr int ROT = 16;
constexpr int PACE = 890;
constexpr int STAG = PACE / 4;
constexpr int LEAD = 2;
constexpr int WIN = 12;
constexpr int SLIP = 35;
constexpr float SCALE = 0.088388347648318440f;
constexpr float LOG2E = 1.4426950408889634f;
constexpr unsigned OOB = 0x7fff0000u;
constexpr unsigned GR_OFF = 65536, GR_ACC = 1024, GR_HEAD = GR_ACC + NSPLIT * 1024;

using bf16x2 = __attribute__((ext_vector_type(2))) __bf16;
using bf16x8 = __attribute__((ext_vector_type(8))) __bf16;
using u32x4 = __attribute__((ext_vector_type(4))) unsigned;
using f32x4 = __attribute__((ext_vector_type(4))) float;
using f32x2 = __attribute__((ext_vector_type(2))) float;
#define SB() __builtin_amdgcn_sched_barrier(0)
#define WG_BARRIER() asm volatile("s_waitcnt lgkmcnt(0)\n\ts_barrier" ::: "memory")

__device__ __forceinline__ float dot2(unsigned k, unsigned q, float acc) {
  return __builtin_amdgcn_fdot2_f32_bf16(__builtin_bit_cast(bf16x2, k), __builtin_bit_cast(bf16x2, q), acc, false);
}
__device__ __forceinline__ float dot8(u32x4 k, u32x4 q) {
  return dot2(k[3], q[3], dot2(k[2], q[2], dot2(k[1], q[1], dot2(k[0], q[0], 0.f))));
}
template <int CTRL> __device__ __forceinline__ float dpp(float x) {
  return __builtin_bit_cast(float, __builtin_amdgcn_mov_dpp(__builtin_bit_cast(int, x), CTRL, 0xf, 0xf, true));
}
template <int CTRL> __device__ __forceinline__ unsigned dppu(unsigned x) {
  return (unsigned)__builtin_amdgcn_mov_dpp((int)x, CTRL, 0xf, 0xf, true);
}
constexpr int XOR1 = 0xB1, XOR2 = 0x4E, XOR7 = 0x141, XOR8 = 0x128;
__device__ __forceinline__ float wave_max(float x) {
  x = fmaxf(x, dpp<XOR1>(x)); x = fmaxf(x, dpp<XOR2>(x)); x = fmaxf(x, dpp<XOR7>(x)); x = fmaxf(x, dpp<XOR8>(x));
  auto s = __builtin_amdgcn_permlane16_swap(__float_as_uint(x), __float_as_uint(x), false, false);
  x = fmaxf(__uint_as_float(s[0]), __uint_as_float(s[1]));
  auto t = __builtin_amdgcn_permlane32_swap(__float_as_uint(x), __float_as_uint(x), false, false);
  return fmaxf(__uint_as_float(t[0]), __uint_as_float(t[1]));
}
__device__ __forceinline__ float wave_sum(float x) {
  x += dpp<XOR1>(x); x += dpp<XOR2>(x); x += dpp<XOR7>(x); x += dpp<XOR8>(x);
  auto s = __builtin_amdgcn_permlane16_swap(__float_as_uint(x), __float_as_uint(x), false, false);
  x = __uint_as_float(s[0]) + __uint_as_float(s[1]);
  auto t = __builtin_amdgcn_permlane32_swap(__float_as_uint(x), __float_as_uint(x), false, false);
  return __uint_as_float(t[0]) + __uint_as_float(t[1]);
}
__device__ __forceinline__ unsigned bf16_rne(float x) {
  return (unsigned)__builtin_bit_cast(unsigned short, (__bf16)x);
}
__device__ __forceinline__ unsigned pack_p(float p) {
  const unsigned hi = bf16_rne(p);
  const unsigned lo = bf16_rne(p - __builtin_bit_cast(float, hi << 16));
  return hi | (lo << 16);
}
__device__ __forceinline__ u32x4 srd(const void* p, unsigned bytes) {
  const unsigned long long a = (unsigned long long)p;
  return u32x4{(unsigned)a, (unsigned)(a >> 32) & 0xffffu, bytes, 0x00020000u};
}
template <int OFF> __device__ __forceinline__ void ld_nt(u32x4& r, unsigned voff, u32x4 rs, unsigned soff) {
  asm volatile("buffer_load_dwordx4 %0, %1, %2, %3 offen offset:%c4 nt" : "+v"(r) : "v"(voff), "s"(rs), "s"(soff), "i"(OFF) : "memory");
}
__device__ __forceinline__ void ld_sc1(u32x4& r, unsigned voff, u32x4 rs, unsigned soff) {
  asm volatile("buffer_load_dwordx4 %0, %1, %2, %3 offen sc1" : "+v"(r) : "v"(voff), "s"(rs), "s"(soff) : "memory");
}
__device__ __forceinline__ void st_sc1(u32x4 x, unsigned voff, u32x4 rs, unsigned soff) {
  asm volatile("buffer_store_dwordx4 %0, %1, %2, %3 offen sc1\n\ts_nop 1" :: "v"(x), "v"(voff), "s"(rs), "s"(soff) : "memory");
}
template <int N> __device__ __forceinline__ void wait4(u32x4& r) { asm volatile("s_waitcnt vmcnt(%c1)" : "+v"(r) : "i"(N) : "memory"); }
__device__ __forceinline__ void pin3(unsigned& a, float& b, float& c) { asm volatile("" : "+v"(a), "+v"(b), "+v"(c)); }
__device__ __forceinline__ void undef4(u32x4& r) { asm volatile("" : "=v"(r)); }
__device__ __forceinline__ void keep4(u32x4& r) { asm volatile("" : "+v"(r)); }

constexpr int brev4(int t) { return ((t & 1) << 3) | ((t & 2) << 1) | ((t & 4) >> 1) | ((t & 8) >> 3); }
template <int LN, int N> __device__ __forceinline__ void pv_step(f32x4& acc, unsigned pk, u32x4& v, const unsigned (&sel)[4]) {
  const unsigned pb = dppu<0x150 + LN>(pk);
  u32x4 b;
#pragma unroll
  for (int w = 0; w < 4; ++w) b[w] = __builtin_amdgcn_perm(pb, pb, sel[w]);
  keep4(b);
  wait4<N>(v);
  acc = __builtin_amdgcn_mfma_f32_16x16x32_bf16(__builtin_bit_cast(bf16x8, v), __builtin_bit_cast(bf16x8, b), acc, 0, 0, 0);
  SB();
}
template <int T> struct IC { static constexpr int v = T; };
template <int N, class F> __device__ __forceinline__ void sfor(F&& f) {
  if constexpr (N > 0) { sfor<N - 1>(f); f(IC<N - 1>{}); }
}

}

__global__ void __launch_bounds__(WAVES * 64)
tree_attn_decode_kernel(const void* q, const void* kc, const void* vc, unsigned char* ws, const void* kt, const void* vt,
                        const void* mask, void* out) {
  __shared__ __attribute__((aligned(16))) float lds[WAVES * 128 + WAVES * NG * 64 + 8];
  const int s = blockIdx.x % NSPLIT, h = blockIdx.x / NSPLIT;
  const int tid = threadIdx.x, lane = tid & 63, wave = __builtin_amdgcn_readfirstlane(tid >> 6), c = lane & 15;
  const int w = s * WAVES + wave;
  const unsigned hb = (unsigned)h * (C * D * 2);
  const unsigned voff = lane * 16;
  auto toffs = [&](int i) -> unsigned { return hb + (unsigned)((i * NW + w + h * ROT) & (NT - 1)) * 4096u; };
  const u32x4 krs = srd(kc, H * C * D * 2), vrs = srd(vc, H * C * D * 2);
  u32x4 qv, r[RING];
  asm volatile("buffer_load_dwordx4 %0, %1, %2, %3 offen" : "=v"(qv) : "v"(c * 16), "s"(srd(q, H * D * 2)), "s"(h * (D * 2)) : "memory");
  unsigned tz = (unsigned)__builtin_amdgcn_s_memrealtime() + ((wave * STAG) >> 4);
  auto pace = [&](int k) {
    asm volatile("s_waitcnt vmcnt(%c0)" :: "i"(WIN - 4) : "memory");
    const unsigned due = tz + (unsigned)(((k - LEAD + 1) * PACE) >> 4);
    int late;
    while ((late = (int)((unsigned)__builtin_amdgcn_s_memrealtime() - due)) < 0) __builtin_amdgcn_s_sleep(1);
    if (k == LEAD && late > SLIP) tz += late - SLIP;
  };
  sfor<LEAD * 4>([&](auto I) { undef4(r[I.v]); ld_nt<(I.v & 3) * 1024>(r[I.v], voff, krs, toffs(I.v >> 2)); });
  const bool cons = s == 0 && wave == 0;
  const u32x4 ers = srd(ws + 512 * h, 8);
  unsigned long long epoch;
  u32x4 ktv, ml;
  unsigned vtv, mkv;
  asm volatile("buffer_load_dwordx2 %0, off, %1, 0 sc1" : "=v"(epoch) : "s"(ers) : "memory");
  asm volatile("" : "=v"(ktv), "=v"(ml), "=v"(vtv), "=v"(mkv));
  if (cons) {
    asm volatile("buffer_load_dwordx4 %0, %1, %2, %3 offen" : "+v"(ktv) : "v"(c * 16), "s"(srd(kt, H * D * 2)), "s"(h * (D * 2)) : "memory");
    asm volatile("buffer_load_dword %0, %1, %2, %3 offen" : "+v"(vtv) : "v"(lane * 4), "s"(srd(vt, H * D * 2)), "s"(h * (D * 2)) : "memory");
    asm volatile("buffer_load_ushort %0, off, %1, 0" : "+v"(mkv) : "s"(srd(mask, 2)) : "memory");
  }
  sfor<RING - LEAD * 4>([&](auto I) {
    constexpr int i = I.v + LEAD * 4;
    if constexpr ((i & 3) == 0) pace(i >> 2);
    undef4(r[i]);
    ld_nt<(i & 3) * 1024>(r[i], voff, krs, toffs(i >> 2));
  });
  asm volatile("s_waitcnt vmcnt(%c1)" : "+v"(qv) : "i"(RING) : "memory");
  float* sco = lds + WAVES * 128 + wave * (NG * 64) + lane;
  float* wmx = lds + WAVES * 128 + WAVES * NG * 64;
  float mx = -INFINITY;
#pragma unroll 1
  for (int g = 0; g < NG; ++g) {
    float xa = 0.f, ya = 0.f, za = 0.f, ua = 0.f, sc = 0.f;
    sfor<16>([&](auto I) {
      constexpr int t = I.v;
      wait4<RING - 1>(r[t % RING]);
      float d = dot8(r[t % RING], qv);
      asm volatile("" : "+v"(d));
      const int ti = g * 4 + (t >> 2) + AHEAD;
      const bool isv = ti >= NTW;
      if constexpr ((t & 3) == 0) pace(ti);
      ld_nt<(t & 3) * 1024>(r[t % RING], voff, isv ? vrs : krs, toffs(isv ? ti - NTW : ti));
      d += dpp<XOR8>(d);
      if constexpr (!(t & 1)) xa = d;
      else {
        float e = (c & 8) ? d : xa;
        e += dpp<XOR7>(e);
        if constexpr (!(t & 2)) ya = e;
        else {
          e = (c & 4) ? e : ya;
          e += dpp<XOR2>(e);
          if constexpr (!(t & 4)) za = e;
          else {
            e = (c & 2) ? e : za;
            e += dpp<XOR1>(e);
            if constexpr (!(t & 8)) ua = e;
            else sc = ((c & 1) ? e : ua) * (SCALE * LOG2E);
          }
        }
      }
    });
    mx = fmaxf(mx, sc);
    sco[g * 64] = sc;
  }
  asm volatile("" : "+v"(epoch), "+v"(ktv), "+v"(vtv), "+v"(mkv));
  mx = wave_max(mx);
  mx = (mx == -INFINITY) ? -3.0e38f : mx;
  float l = 0.f;
#pragma unroll
  for (int g = 0; g < NG; ++g) l += __builtin_amdgcn_exp2f(sco[g * 64] - mx);
  l = wave_sum(l);
  if (lane == 0) { wmx[wave] = mx; wmx[4 + wave] = l; }
  const unsigned tagA = (unsigned)(epoch + 1ull), tagB = (unsigned)((epoch + 1ull) >> 32) ^ (tagA * 0x9E3779B1u);
  const u32x4 gsr = srd(ws + GR_OFF + h * GR_HEAD, GR_HEAD);
  unsigned sel[4];
  {
    const unsigned b = (c & 8) ? 0x0302u : 0x0100u;
    const unsigned sv = (c & 1) ? ((b << 16) | 0x0c0cu) : (0x0c0c0000u | b);
#pragma unroll
    for (int i = 0; i < 4; ++i) sel[i] = (((c >> 1) & 3) == i) ? sv : 0x0c0c0c0cu;
  }
  f32x4 acc = {0.f, 0.f, 0.f, 0.f};
#pragma unroll 1
  for (int g = 0; g < NG - 1; ++g) {
    const unsigned pk = pack_p(__builtin_amdgcn_exp2f(sco[g * 64] - mx));
    sfor<16>([&](auto I) {
      constexpr int t = I.v;
      pv_step<brev4(t), RING - 1>(acc, pk, r[t % RING], sel);
      const int ti = g * 4 + (t >> 2) + AHEAD;
      if constexpr ((t & 3) == 0) pace(NTW + ti);
      ld_nt<(t & 3) * 1024>(r[t % RING], voff, vrs, toffs(ti));
    });
  }
  {
    const float sl = sco[(NG - 1) * 64];
    WG_BARRIER();
    const f32x4 m4 = *reinterpret_cast<const f32x4*>(wmx), l4 = *reinterpret_cast<const f32x4*>(wmx + 4);
    const float M = fmaxf(fmaxf(m4[0], m4[1]), fmaxf(m4[2], m4[3]));
    float L = 0.f;
#pragma unroll
    for (int i = 0; i < WAVES; ++i) L = fmaf(__builtin_amdgcn_exp2f(m4[i] - M), l4[i], L);
    st_sc1(u32x4{tagA, __float_as_uint(M), tagB, __float_as_uint(L)}, tid == 0 ? 0u : OOB, gsr, s * 16);
    acc *= __builtin_amdgcn_exp2f(mx - M);
    const unsigned pk = pack_p(__builtin_amdgcn_exp2f(sl - M));
    sfor<16>([&](auto I) { pv_step<brev4(I.v), 16 - I.v>(acc, pk, r[I.v], sel); });
  }
  {
    const bool up = c & 8;
    const float s0 = up ? acc[0] : acc[2], s1 = up ? acc[1] : acc[3];
    const float e0 = (up ? acc[2] : acc[0]) + dpp<XOR8>(s0), e1 = (up ? acc[3] : acc[1]) + dpp<XOR8>(s1);
    float* rec = lds + wave * 128 + 32 * (lane >> 4) + 2 * (c & 8) + (c & 7);
    rec[0] = e0; rec[8] = e1;
  }
  u32x4 av[NSPLIT], bv[NSPLIT];
  sfor<NSPLIT>([&](auto J) { undef4(av[J.v]); undef4(bv[J.v]); });
  auto sweep = [&](u32x4 (&x)[NSPLIT]) {
    sfor<NSPLIT - 1>([&](auto J) { ld_sc1(x[J.v + 1], voff, gsr, GR_ACC + (J.v + 1) * 1024); });
  };
  if (cons) { ld_sc1(ml, (lane & (NSPLIT - 1)) * 16, gsr, 0); sweep(av); }
  WG_BARRIER();
  if (wave != 0) return;
  f32x2 o = *reinterpret_cast<const f32x2*>(lds + 2 * lane);
#pragma unroll
  for (int i = 1; i < WAVES; ++i) o += *reinterpret_cast<const f32x2*>(lds + i * 128 + 2 * lane);
  if (s != 0) {
    st_sc1(u32x4{tagA, __float_as_uint(o[0]), tagB, __float_as_uint(o[1])}, voff, gsr, GR_ACC + s * 1024);
    return;
  }
  float dt = dot8(ktv, qv);
  dt += dpp<XOR1>(dt); dt += dpp<XOR2>(dt); dt += dpp<XOR7>(dt); dt += dpp<XOR8>(dt);
  const float mk = __builtin_bit_cast(float, mkv << 16);
  const float st = (dt * SCALE + (1.0f - mk) * -1000000000.0f) * LOG2E;
  wait4<NSPLIT - 1>(ml);
  {
    unsigned spins = 0;
    while (__builtin_expect(!__all((ml[0] == tagA) & (ml[2] == tagB)), 0) && ++spins < (1u << 18)) {
      ld_sc1(ml, (lane & (NSPLIT - 1)) * 16, gsr, 0);
      wait4<0>(ml);
    }
  }
  const float mj = __uint_as_float(ml[1]);
  float Mf = fmaxf(mj, st);
  Mf = fmaxf(Mf, dpp<XOR1>(Mf)); Mf = fmaxf(Mf, dpp<XOR2>(Mf)); Mf = fmaxf(Mf, dpp<XOR7>(Mf));
  float wj = __builtin_amdgcn_exp2f(mj - Mf), wt = __builtin_amdgcn_exp2f(st - Mf);
  float Lf = wj * __uint_as_float(ml[3]);
  Lf += dpp<XOR1>(Lf); Lf += dpp<XOR2>(Lf); Lf += dpp<XOR7>(Lf);
  const float inv = 1.0f / (Lf + wt);
  wj *= inv; wt *= inv;
  float wsp[NSPLIT];
#pragma unroll
  for (int j = 0; j < NSPLIT; ++j) wsp[j] = __uint_as_float(__builtin_amdgcn_readlane(__float_as_uint(wj), j));
  const float t0 = wt * __builtin_bit_cast(float, vtv << 16), t1 = wt * __builtin_bit_cast(float, vtv & 0xffff0000u);
  const float b0 = fmaf(wsp[0], o[0], t0), b1 = fmaf(wsp[0], o[1], t1);
  float f0 = 0.f, f1 = 0.f;
  auto take = [&](u32x4 (&x)[NSPLIT]) -> bool {
    unsigned bad = 0;
    f0 = b0; f1 = b1;
    sfor<NSPLIT - 1>([&](auto J) {
      constexpr int j = J.v + 1;
      wait4<2 * (NSPLIT - 1) - j>(x[j]);
      bad |= (x[j][0] ^ tagA) | (x[j][2] ^ tagB);
      f0 = fmaf(wsp[j], __uint_as_float(x[j][1]), f0);
      f1 = fmaf(wsp[j], __uint_as_float(x[j][3]), f1);
      pin3(bad, f0, f1);
    });
    return !__any(bad != 0);
  };
  for (unsigned spins = 0; spins < (1u << 17); ++spins) {
    sweep(bv);
    if (take(av)) break;
    sweep(av);
    if (take(bv)) break;
  }
  asm volatile("buffer_store_dword %0, %1, %2, %3 offen sc1"
               :: "v"(bf16_rne(f0) | (bf16_rne(f1) << 16)), "v"(lane * 4), "s"(srd(out, H * D * 2)), "s"(h * (D * 2)) : "memory");
  asm volatile("buffer_store_dwordx2 %0, %1, %2, 0 offen sc1" :: "v"(epoch + 1ull), "v"(lane == 0 ? 0u : OOB), "s"(ers) : "memory");
  sfor<NSPLIT - 1>([&](auto J) { keep4(av[J.v + 1]); keep4(bv[J.v + 1]); });
}

extern "C" void kernel_launch(void* const* d_in, const int* in_sizes, int n_in, void* d_out, int out_size, void* d_ws,
                              size_t ws_size, hipStream_t stream) {
  tree_attn_decode_kernel<<<H * NSPLIT, WAVES * 64, 0, stream>>>(d_in[0], d_in[1], d_in[2], (unsigned char*)d_ws, d_in[3],
                                                                 d_in[4], d_in[5], d_out);
}
```
